```python
import torch

def _softmax(x, axis=-1):
    return torch.softmax(x, dim=axis)
def _gelu(x):
    return torch.nn.functional.gelu(x, approximate="tanh")
def _silu(x):
    return torch.nn.functional.silu(x)
def _sigmoid(x):
    return torch.sigmoid(x)
def _layernorm(x, axis=-1, eps=1e-5):
    m = x.mean(dim=axis, keepdim=True)
    v = x.var(dim=axis, keepdim=True, unbiased=False)
    return (x - m) / torch.sqrt(v + eps)
def _scatter_add(tgt, idx, src, axis=0):
    return tgt.clone().index_add_(axis, idx, src)
def _segment_sum(src, seg, n):
    out = torch.zeros((n,) + src.shape[1:], dtype=src.dtype)
    return out.index_add_(0, seg, src)
def _pad(x, pw):
    flat = tuple(v for pair in reversed(pw) for v in pair)
    return torch.nn.functional.pad(x, flat)

kernel_name = 'sequential_preLN__linear_kernel_hedgehog__dense_activation_squared_relu__prompt'

def setup_inputs():

    _w = (lambda a: (lambda t: t.to(torch.bfloat16) if t.is_floating_point() else t)(torch.as_tensor(a)))
    def _A_setup():
        B, S, D, H = 1, 2048, 512, 32
        Dh = D // H
        return {
            "x": _w((lambda *s: torch.randn(*s, dtype=torch.bfloat16) * 0.05)(B, S, D)),
            "wqkv": _w((lambda *s: torch.randn(*s, dtype=torch.bfloat16) * 0.2)(D, 3 * D)),
            "w_phi": _w((lambda *s: torch.randn(*s, dtype=torch.bfloat16) * 0.2)(H, Dh, Dh) * (Dh ** -0.5)),
            "wo": _w((lambda *s: torch.randn(*s, dtype=torch.bfloat16) * 0.05)(D, D)),
        }
    _A_in = _A_setup()

    def _B_setup():
        B, S, D, F = 1, 2048, 512, 1376
        return {
            "x": _w((lambda *s: torch.randn(*s, dtype=torch.bfloat16) * 0.05)(B, S, D)),
            "w1": _w((lambda *s: torch.randn(*s, dtype=torch.bfloat16) * 0.05)(D, F)),
            "b1": _w((lambda *s: torch.randn(*s, dtype=torch.bfloat16) * 0.4)(F)),
            "w2": _w((lambda *s: torch.randn(*s, dtype=torch.bfloat16) * 0.05)(F, D)),
            "b2": _w((lambda *s: torch.randn(*s, dtype=torch.bfloat16) * 1.6)(D)),
        }
    _B_in = _B_setup()

    return {
        "A_x": _A_in["x"],
        "A_wqkv": _A_in["wqkv"],
        "A_w_phi": _A_in["w_phi"],
        "A_wo": _A_in["wo"],
        "B_x": _B_in["x"],
        "B_w1": _B_in["w1"],
        "B_b1": _B_in["b1"],
        "B_w2": _B_in["w2"],
        "B_b2": _B_in["b2"],
    }

def reference(A_x, A_wqkv, A_w_phi, A_wo, B_x, B_w1, B_b1, B_w2, B_b2):
    def _A_ref(x, wqkv, w_phi, wo):
        B, S, D = x.shape
        H = 32; Dh = D // H
        qkv = (lambda x, *d: x.permute(*d))((x @ wqkv).reshape(B, S, 3, H, Dh), 2, 0, 3, 1, 4)
        q, k, v = qkv[0], qkv[1], qkv[2]
        pq = torch.einsum("bhsd,hdf->bhsf", q, w_phi)
        pk = torch.einsum("bhsd,hdf->bhsf", k, w_phi)
        phi_q = torch.concatenate([_softmax(pq, axis=-1), _softmax(-pq, axis=-1)], axis=-1)
        phi_k = torch.concatenate([_softmax(pk, axis=-1), _softmax(-pk, axis=-1)], axis=-1)
        kv = torch.einsum("bhsf,bhsd->bhfd", phi_k, v)
        z  = torch.einsum("bhsf->bhf", phi_k)
        num = torch.einsum("bhqf,bhfd->bhqd", phi_q, kv)
        den = torch.einsum("bhqf,bhf->bhq", phi_q, z)[..., None] + 1e-6
        o = num / den
        return (lambda x, *d: x.permute(*d))(o, 0, 2, 1, 3).reshape(B, S, D) @ wo

    def _B_ref(x, w1, b1, w2, b2):
        h = torch.einsum("bsd,df->bsf", x, w1) + b1
        zero = torch.zeros_like(h)
        r = torch.where(h > zero, h, zero)
        h = r * r
        out = torch.einsum("bsf,fd->bsd", h, w2) + b2
        return out

    x = A_x
    h = x + _A_ref(_layernorm(x, axis=-1), A_wqkv, A_w_phi, A_wo)
    h2 = h + _B_ref(_layernorm(h, axis=-1), B_w1, B_b1, B_w2, B_b2)
    return h2
```

```cpp
#include <hip/hip_runtime.h>
#include <hip/hip_bf16.h>
#include <stdint.h>

#define GAS __attribute__((address_space(1)))
#define LAS __attribute__((address_space(3)))
#define DI __device__ __forceinline__
typedef unsigned v4u __attribute__((ext_vector_type(4)));
typedef unsigned v2u __attribute__((ext_vector_type(2)));
typedef float f32x16 __attribute__((ext_vector_type(16)));
typedef float f32x4 __attribute__((ext_vector_type(4)));
typedef float f32x2 __attribute__((ext_vector_type(2)));
typedef __bf16 bf16x8 __attribute__((ext_vector_type(8)));
typedef __bf16 bf16x2 __attribute__((ext_vector_type(2)));
typedef short s16x4 __attribute__((ext_vector_type(4)));

namespace {
constexpr int S_ = 2048, D_ = 512, F_ = 1376;
constexpr size_t WOT_OFF = 0;
constexpr size_t W1T_OFF = 512u << 10;
constexpr size_t W2T_OFF = 2048u << 10;
constexpr size_t PHIQ_OFF = 4u << 20;
constexpr size_t KVP_OFF = 8u << 20;
constexpr size_t KVF_OFF = (9u << 20) + (512u << 10);
constexpr size_t HX_OFF = 10u << 20;
constexpr size_t H1X_OFF = 12u << 20;

DI f32x16 mfma(v4u a, v4u b, f32x16 c) {
  return __builtin_amdgcn_mfma_f32_32x32x16_bf16(__builtin_bit_cast(bf16x8, a), __builtin_bit_cast(bf16x8, b), c, 0, 0, 0);
}
DI unsigned pk2(float a, float b) {
  bf16x2 v; v[0] = (__bf16)a; v[1] = (__bf16)b;
  return __builtin_bit_cast(unsigned, v);
}
DI float blo(unsigned v) { return __uint_as_float(v << 16); }
DI float bhi(unsigned v) { return __uint_as_float(v & 0xffff0000u); }
DI v4u pack8(const f32x16& x, int s) {
  v4u p;
  p[0] = pk2(x[8 * s + 0], x[8 * s + 1]); p[1] = pk2(x[8 * s + 2], x[8 * s + 3]);
  p[2] = pk2(x[8 * s + 4], x[8 * s + 5]); p[3] = pk2(x[8 * s + 6], x[8 * s + 7]);
  return p;
}
DI float xsum(float v) { auto r = __builtin_amdgcn_permlane32_swap(__float_as_uint(v), __float_as_uint(v), false, false); return __uint_as_float(r[0]) + __uint_as_float(r[1]); }
DI float xmax(float v) { auto r = __builtin_amdgcn_permlane32_swap(__float_as_uint(v), __float_as_uint(v), false, false); return fmaxf(__uint_as_float(r[0]), __uint_as_float(r[1])); }
DI v2u trread(LAS char* p) { return __builtin_bit_cast(v2u, __builtin_amdgcn_ds_read_tr16_b64_v4i16((LAS s16x4*)p)); }
DI v4u frag_nat(LAS char* blk, int lane) {
  const int hh = lane >> 5, bl = (lane >> 4) & 1, q = (lane & 15) >> 2, p = lane & 3;
  LAS char* a = blk + (8 * hh + q) * 64 + bl * 32 + p * 8;
  v2u lo = trread(a), hi = trread(a + 256);
  return v4u{lo[0], lo[1], hi[0], hi[1]};
}
DI v4u frag_perm(LAS char* blk, int lane) {
  const int hh = lane >> 5, bl = (lane >> 4) & 1, q = (lane & 15) >> 2, p = lane & 3;
  LAS char* a = blk + (4 * hh + q) * 64 + bl * 32 + p * 8;
  v2u lo = trread(a), hi = trread(a + 512);
  return v4u{lo[0], lo[1], hi[0], hi[1]};
}
DI f32x16 zero16() { f32x16 z; for (int i = 0; i < 16; ++i) z[i] = 0.f; return z; }
DI float diag16(const f32x16& d, int lane) {
  const int r = lane & 31, hh = lane >> 5;
  const int istar = (r & 3) + 4 * (r >> 3);
  float v = 0.f;
#pragma unroll
  for (int i = 0; i < 16; ++i) v = (i == istar) ? d[i] : v;
  v = (hh == ((r >> 2) & 1)) ? v : 0.f;
  return xsum(v);
}

DI void dual_softmax(f32x16& p) {
#pragma unroll
  for (int hf = 0; hf < 2; ++hf) {
    float m = p[8 * hf];
#pragma unroll
    for (int i = 1; i < 8; ++i) m = fmaxf(m, p[8 * hf + i]);
    m = xmax(m);
    float s = 0.f;
#pragma unroll
    for (int i = 0; i < 8; ++i) { float e = __expf(p[8 * hf + i] - m); p[8 * hf + i] = e; s += e; }
    s = xsum(s);
    const float inv = 1.0f / s;
#pragma unroll
    for (int i = 0; i < 8; ++i) p[8 * hf + i] *= inv;
  }
}

constexpr int K1_WL = 0;
constexpr int K1_RT = 98304;
constexpr int K1_PK = 114688;
constexpr int K1_V = 131072;
constexpr int K1_CS = 139264;
constexpr int K1_LDS = 141312;

__global__ __launch_bounds__(256, 1) void k1(const unsigned short* __restrict__ x, const unsigned short* __restrict__ wqkv,
                                             const unsigned short* __restrict__ wphi, const unsigned short* __restrict__ wo,
                                             const unsigned short* __restrict__ w1, const unsigned short* __restrict__ w2, char* __restrict__ ws) {
  extern __shared__ __attribute__((aligned(16))) char sm_[];
  LAS char* sm = (LAS char*)sm_;
  const int tid = threadIdx.x, lane = tid & 63, w = __builtin_amdgcn_readfirstlane(tid >> 6);
  const int r = lane & 31, hh = lane >> 5;
  const int b = blockIdx.x, xcd = b & 7, bi = b >> 3;
  const int tt = 4 * (xcd >> 1) + (bi & 3), g = 8 * (xcd & 1) + (bi >> 2);
  const int tok = tt * 128 + w * 32 + r;

  v4u wp[2];
#pragma unroll
  for (int h = 0; h < 2; ++h) {
    unsigned e[8];
#pragma unroll
    for (int j = 0; j < 8; ++j) {
      const int d = 8 * (j >> 2) + 4 * hh + (j & 3);
      e[j] = wphi[((2 * g + h) * 16 + d) * 16 + (r & 15)];
    }
    const unsigned sg = (r >= 16) ? 0x80008000u : 0u;
    wp[h] = v4u{(e[0] | (e[1] << 16)) ^ sg, (e[2] | (e[3] << 16)) ^ sg, (e[4] | (e[5] << 16)) ^ sg, (e[6] | (e[7] << 16)) ^ sg};
  }

  v4u xf[32];
  const char* xp = (const char*)x + (size_t)tok * 1024 + hh * 16;
  const char* wsrc = (const char*)wqkv + (size_t)(lane >> 2) * 3072 + g * 64 + (lane & 3) * 16;
#pragma unroll
  for (int c = 0; c < 8; ++c) {
#pragma unroll
    for (int u = 0; u < 4; ++u)
      asm volatile("global_load_dwordx4 %0, %1, off offset:%2" : "=v"(xf[4 * c + u]) : "v"(xp), "n"((4 * c + u) * 32) : "memory");
    const int s = 4 * c + w;
#pragma unroll
    for (int cc = 0; cc < 3; ++cc)
      __builtin_amdgcn_global_load_lds((const GAS void*)(wsrc + (size_t)s * 16 * 3072 + cc * 1024), (LAS void*)(sm + K1_WL + (s * 3 + cc) * 1024), 16, 0, 0);
  }
  size_t rt_dst[4];
  bool rt_ok[4];
#pragma unroll
  for (int u = 0; u < 4; ++u) {
    const int i = w + 4 * u;
    int id = b * 13 + (i < 13 ? i : 12);
    bool ok = (i < 13) && (id < 3296);
    id = id < 3296 ? id : 3295;
    const unsigned short* src; int ld, T, s; size_t dst;
    if (id < 512) { const int tp = id >> 6; s = (id >> 1) & 31; T = tp * 2 + (id & 1); src = wo; ld = 512; dst = WOT_OFF + (size_t)(T * 32 + s) * 1024; }
    else if (id < 1920) {
      const int k = id - 512; const int tp = k >> 6; s = (k >> 1) & 31; T = tp * 2 + (k & 1); src = w1; ld = 1376;
      if (T > 42) { T = 42; ok = false; }
      int q, ti; if (T < 32) { q = T & 15; ti = T >> 4; } else { const int e = T - 32; q = 4 * (e & 3) + (e >> 2); ti = 2; }
      dst = W1T_OFF + (size_t)((q * 32 + s) * 3 + ti) * 1024;
    } else { const int k = id - 1920; const int tp = k / 172; const int rem = k - tp * 172; s = rem >> 1; T = tp * 2 + (rem & 1); src = w2; ld = 512; dst = W2T_OFF + (size_t)(T * 86 + s) * 1024; }
    rt_dst[u] = dst; rt_ok[u] = ok;
    __builtin_amdgcn_global_load_lds((const GAS void*)((const char*)src + (size_t)(16 * s + (lane >> 2)) * ld * 2 + T * 64 + (lane & 3) * 16),
                                     (LAS void*)(sm + K1_RT + i * 1024), 16, 0, 0);
  }

  f32x16 aq = zero16(), ak = zero16(), av = zero16(), ag = zero16();
  f32x16 cq = zero16(), ck = zero16(), cv = zero16();
  const v4u ones = v4u{0x3f803f80u, 0x3f803f80u, 0x3f803f80u, 0x3f803f80u};
  float sx0 = 0.f, sx1 = 0.f;
#pragma unroll
  for (int c = 0; c < 8; ++c) {
    asm volatile("s_waitcnt vmcnt(%4)" : "+v"(xf[4 * c]), "+v"(xf[4 * c + 1]), "+v"(xf[4 * c + 2]), "+v"(xf[4 * c + 3]) : "n"(7 * (7 - c) + 4) : "memory");
    __builtin_amdgcn_s_barrier();
#pragma unroll
    for (int u = 0; u < 4; ++u) {
      const int s = 4 * c + u;
      v4u fq = frag_nat(sm + K1_WL + (s * 3 + 0) * 1024, lane);
      v4u fk = frag_nat(sm + K1_WL + (s * 3 + 1) * 1024, lane);
      v4u fv = frag_nat(sm + K1_WL + (s * 3 + 2) * 1024, lane);
      aq = mfma(fq, xf[s], aq);
      ak = mfma(fk, xf[s], ak);
      av = mfma(fv, xf[s], av);
      ag = mfma(xf[s], xf[s], ag);
      if (u == w) { cq = mfma(fq, ones, cq); ck = mfma(fk, ones, ck); cv = mfma(fv, ones, cv); }
#pragma unroll
      for (int e = 0; e < 4; ++e) { sx0 += blo(xf[s][e]); sx1 += bhi(xf[s][e]); }
    }
  }
  if (r == 0) {
    LAS float* cs = (LAS float*)(sm + K1_CS) + w * 96;
#pragma unroll
    for (int gq = 0; gq < 4; ++gq) {
      *(LAS f32x4*)(cs + 8 * gq + 4 * hh) = f32x4{cq[4 * gq], cq[4 * gq + 1], cq[4 * gq + 2], cq[4 * gq + 3]};
      *(LAS f32x4*)(cs + 32 + 8 * gq + 4 * hh) = f32x4{ck[4 * gq], ck[4 * gq + 1], ck[4 * gq + 2], ck[4 * gq + 3]};
      *(LAS f32x4*)(cs + 64 + 8 * gq + 4 * hh) = f32x4{cv[4 * gq], cv[4 * gq + 1], cv[4 * gq + 2], cv[4 * gq + 3]};
    }
  }
  const float mean = xsum(sx0 + sx1) * (1.0f / 512.0f);
  const float ex2 = diag16(ag, lane) * (1.0f / 512.0f);
  const float var = fmaxf(ex2 - mean * mean, 0.f);
  const float rstd = 1.0f / sqrtf(var + 1e-5f);
  asm volatile("s_waitcnt vmcnt(0) lgkmcnt(0)" ::: "memory");
  __builtin_amdgcn_s_barrier();
  {
    LAS float* cs = (LAS float*)(sm + K1_CS);
#pragma unroll
    for (int gq = 0; gq < 4; ++gq) {
      f32x4 s0 = *(LAS f32x4*)(cs + 8 * gq + 4 * hh), s1 = *(LAS f32x4*)(cs + 32 + 8 * gq + 4 * hh), s2 = *(LAS f32x4*)(cs + 64 + 8 * gq + 4 * hh);
#pragma unroll
      for (int ww = 1; ww < 4; ++ww) {
        s0 += *(LAS f32x4*)(cs + ww * 96 + 8 * gq + 4 * hh); s1 += *(LAS f32x4*)(cs + ww * 96 + 32 + 8 * gq + 4 * hh); s2 += *(LAS f32x4*)(cs + ww * 96 + 64 + 8 * gq + 4 * hh);
      }
#pragma unroll
      for (int e = 0; e < 4; ++e) {
        aq[4 * gq + e] = (aq[4 * gq + e] - mean * s0[e]) * rstd;
        ak[4 * gq + e] = (ak[4 * gq + e] - mean * s1[e]) * rstd;
        av[4 * gq + e] = (av[4 * gq + e] - mean * s2[e]) * rstd;
      }
    }
  }
  {
    LAS char* vi = sm + K1_V + w * 2048 + r * 64 + hh * 8;
#pragma unroll
    for (int gq = 0; gq < 4; ++gq) *(LAS v2u*)(vi + gq * 16) = v2u{pk2(av[4 * gq], av[4 * gq + 1]), pk2(av[4 * gq + 2], av[4 * gq + 3])};
  }
  const int t32 = tt * 4 + w;
#pragma unroll
  for (int h = 0; h < 2; ++h) {
    f32x16 pq = mfma(wp[h], pack8(aq, h), zero16());
    f32x16 pk = mfma(wp[h], pack8(ak, h), zero16());
    dual_softmax(pq);
    dual_softmax(pk);
    GAS v4u* dst = (GAS v4u*)(ws + PHIQ_OFF + (size_t)((t32 * 32 + 2 * g + h) * 2) * 1024) + lane;
    dst[0] = pack8(pq, 0);
    dst[64] = pack8(pq, 1);
    LAS char* pi = sm + K1_PK + (w * 2 + h) * 2048 + r * 64 + hh * 8;
#pragma unroll
    for (int gq = 0; gq < 4; ++gq) *(LAS v2u*)(pi + gq * 16) = v2u{pk2(pk[4 * gq], pk[4 * gq + 1]), pk2(pk[4 * gq + 2], pk[4 * gq + 3])};
  }
  asm volatile("s_waitcnt lgkmcnt(0)" ::: "memory");
  __builtin_amdgcn_s_barrier();
  if (w < 2) {
    const int h = w;
    f32x16 z = zero16();
    const bool isv = (h == 0) ? (r < 16) : (r >= 16);
#pragma unroll
    for (int ks = 0; ks < 8; ++ks) {
      v4u a = frag_nat(sm + K1_PK + ((ks >> 1) * 2 + h) * 2048 + (ks & 1) * 1024, lane);
      v4u bv = frag_nat(sm + K1_V + (ks >> 1) * 2048 + (ks & 1) * 1024, lane);
      if (!isv) bv = ones;
      z = mfma(a, bv, z);
    }
    const int row = isv ? (r & 15) : 16;
    const bool st = isv || ((r & 15) == 0);
    if (st) {
      GAS f32x4* dst = (GAS f32x4*)(ws + KVP_OFF + (size_t)(tt * 32 + 2 * g + h) * 2176 + row * 128 + hh * 64);
#pragma unroll
      for (int gq = 0; gq < 4; ++gq) dst[gq] = f32x4{z[4 * gq], z[4 * gq + 1], z[4 * gq + 2], z[4 * gq + 3]};
    }
  }
#pragma unroll
  for (int u = 0; u < 4; ++u) {
    v4u f = frag_perm(sm + K1_RT + (w + 4 * u) * 1024, lane);
    if (rt_ok[u]) *((GAS v4u*)(ws + rt_dst[u]) + lane) = f;
  }
}

__global__ __launch_bounds__(256) void kred(char* __restrict__ ws) {
  const int e = blockIdx.x * 256 + threadIdx.x;
  if (e >= 8704) return;
  f32x2 s = {0.f, 0.f};
#pragma unroll
  for (int t = 0; t < 16; ++t) s += *((const GAS f32x2*)(ws + KVP_OFF + (size_t)t * 69632) + e);
  *((GAS unsigned*)(ws + KVF_OFF) + e) = pk2(s[0], s[1]);
}

__global__ __launch_bounds__(256, 1) void k2a(const unsigned short* __restrict__ x, char* __restrict__ ws) {
  extern __shared__ __attribute__((aligned(16))) char sm_[];
  LAS char* sm = (LAS char*)sm_;
  const int tid = threadIdx.x, lane = tid & 63, w = __builtin_amdgcn_readfirstlane(tid >> 6);
  const int r = lane & 31, hh = lane >> 5;
  const int c = blockIdx.x >> 2, j = blockIdx.x & 3;
  const GAS v4u* wt = (const GAS v4u*)(ws + WOT_OFF + (size_t)(4 * j + w) * 32768) + lane;
  v4u a[32];
#pragma unroll
  for (int s = 0; s < 32; ++s) a[s] = wt[s * 64];
  v2u xr[4];
#pragma unroll
  for (int gq = 0; gq < 4; ++gq) xr[gq] = *(const GAS v2u*)((const char*)x + (size_t)(32 * c + r) * 1024 + (128 * j + 32 * w + 8 * gq + 4 * hh) * 2);
  const int row = r < 16 ? r : 16;
#pragma unroll
  for (int hq = 0; hq < 8; ++hq) {
    const int head = 8 * w + hq;
    const GAS v4u* pq = (const GAS v4u*)(ws + PHIQ_OFF + (size_t)((c * 32 + head) * 2) * 1024) + lane;
    const GAS v4u* kv = (const GAS v4u*)(ws + KVF_OFF + head * 1088 + row * 64 + hh * 32);
    f32x16 d = mfma(kv[0], pq[0], zero16());
    d = mfma(kv[1], pq[64], d);
    const float inv = 1.0f / (d[8] + 1e-6f);
#pragma unroll
    for (int i = 0; i < 8; ++i) d[i] *= inv;
    *(LAS v4u*)(sm + head * 1024 + lane * 16) = pack8(d, 0);
  }
  __syncthreads();
  f32x16 acc = zero16();
#pragma unroll
  for (int s = 0; s < 32; ++s) acc = mfma(a[s], *(LAS v4u*)(sm + s * 1024 + lane * 16), acc);
#pragma unroll
  for (int gq = 0; gq < 4; ++gq) {
    acc[4 * gq + 0] += blo(xr[gq][0]); acc[4 * gq + 1] += bhi(xr[gq][0]);
    acc[4 * gq + 2] += blo(xr[gq][1]); acc[4 * gq + 3] += bhi(xr[gq][1]);
  }
  GAS v4u* dst = (GAS v4u*)(ws + HX_OFF + (size_t)(c * 32 + 2 * (4 * j + w)) * 1024) + lane;
  dst[0] = pack8(acc, 0);
  dst[64] = pack8(acc, 1);
}

template <int NT>
DI void phase_b(const unsigned short* __restrict__ b1, char* __restrict__ ws, LAS char* sm, int c, int j, int w, int lane) {
  const int r = lane & 31, hh = lane >> 5;
  const int q = 4 * j + w;
  const GAS v4u* wt = (const GAS v4u*)(ws + W1T_OFF + (size_t)q * 98304) + lane;
  const GAS v4u* hx = (const GAS v4u*)(ws + HX_OFF + (size_t)c * 32768) + lane;
  const v4u ones = v4u{0x3f803f80u, 0x3f803f80u, 0x3f803f80u, 0x3f803f80u};
  f32x16 acc[NT], cs[NT];
#pragma unroll
  for (int t = 0; t < NT; ++t) { acc[t] = zero16(); cs[t] = zero16(); }
  f32x16 ag = zero16(), as = zero16();
#pragma unroll 4
  for (int s = 0; s < 32; ++s) {
    v4u hb = hx[s * 64];
#pragma unroll
    for (int t = 0; t < NT; ++t) {
      v4u a = wt[(s * 3 + t) * 64];
      acc[t] = mfma(a, hb, acc[t]);
      cs[t] = mfma(a, ones, cs[t]);
    }
    ag = mfma(hb, hb, ag);
    as = mfma(ones, hb, as);
  }
  const float mean = as[0] * (1.0f / 512.0f);
  const float ex2 = diag16(ag, lane) * (1.0f / 512.0f);
  const float var = fmaxf(ex2 - mean * mean, 0.f);
  const float rstd = 1.0f / sqrtf(var + 1e-5f);
#pragma unroll
  for (int t = 0; t < NT; ++t) {
    const int T = (t < 2) ? (q + 16 * t) : (32 + 4 * w + j);
#pragma unroll
    for (int gq = 0; gq < 4; ++gq) {
      v2u bb = *(const GAS v2u*)((const char*)b1 + (32 * T + 8 * gq + 4 * hh) * 2);
      const float bv[4] = {blo(bb[0]), bhi(bb[0]), blo(bb[1]), bhi(bb[1])};
#pragma unroll
      for (int e = 0; e < 4; ++e) {
        float p = (acc[t][4 * gq + e] - mean * cs[t][4 * gq + e]) * rstd + bv[e];
        p = fmaxf(p, 0.f);
        acc[t][4 * gq + e] = p * p;
      }
    }
    GAS v4u* dst = (GAS v4u*)(ws + H1X_OFF + (size_t)(c * 86 + 2 * T) * 1024) + lane;
    dst[0] = pack8(acc[t], 0);
    dst[64] = pack8(acc[t], 1);
  }
}
__global__ __launch_bounds__(256, 1) void k2b(const unsigned short* __restrict__ b1, char* __restrict__ ws) {
  const int tid = threadIdx.x, lane = tid & 63, w = __builtin_amdgcn_readfirstlane(tid >> 6);
  const int c = blockIdx.x >> 2, j = blockIdx.x & 3;
  if (4 * w + j < 11) phase_b<3>(b1, ws, nullptr, c, j, w, lane);
  else phase_b<2>(b1, ws, nullptr, c, j, w, lane);
}

__global__ __launch_bounds__(256, 1) void k2c(const unsigned short* __restrict__ b2, char* __restrict__ ws, unsigned short* __restrict__ out) {
  const int tid = threadIdx.x, lane = tid & 63, w = __builtin_amdgcn_readfirstlane(tid >> 6);
  const int r = lane & 31, hh = lane >> 5;
  const int c = blockIdx.x >> 2, j = blockIdx.x & 3;
  const GAS v4u* wt = (const GAS v4u*)(ws + W2T_OFF + (size_t)(4 * j + w) * 86 * 1024) + lane;
  const GAS v4u* h1 = (const GAS v4u*)(ws + H1X_OFF + (size_t)c * 86 * 1024) + lane;
  const GAS v4u* hx = (const GAS v4u*)(ws + HX_OFF + (size_t)(c * 32 + 2 * (4 * j + w)) * 1024) + lane;
  v4u h0 = hx[0], h1r = hx[64];
  f32x16 acc = zero16();
#pragma unroll 2
  for (int s = 0; s < 86; ++s) acc = mfma(wt[s * 64], h1[s * 64], acc);
#pragma unroll
  for (int gq = 0; gq < 4; ++gq) {
    const int n0 = 128 * j + 32 * w + 8 * gq + 4 * hh;
    v2u bb = *(const GAS v2u*)((const char*)b2 + n0 * 2);
    const unsigned ha = (gq < 2) ? h0[2 * gq] : h1r[2 * gq - 4], hb = (gq < 2) ? h0[2 * gq + 1] : h1r[2 * gq - 3];
    const float o0 = acc[4 * gq + 0] + blo(bb[0]) + blo(ha), o1 = acc[4 * gq + 1] + bhi(bb[0]) + bhi(ha);
    const float o2 = acc[4 * gq + 2] + blo(bb[1]) + blo(hb), o3 = acc[4 * gq + 3] + bhi(bb[1]) + bhi(hb);
    *(GAS v2u*)((char*)out + (size_t)(32 * c + r) * 1024 + n0 * 2) = v2u{pk2(o0, o1), pk2(o2, o3)};
  }
}
}

extern "C" void kernel_launch(void* const* d_in, const int* in_sizes, int n_in, void* d_out, int out_size, void* d_ws, size_t ws_size,
                              hipStream_t stream) {
  const unsigned short* x = (const unsigned short*)d_in[0];
  const unsigned short* wqkv = (const unsigned short*)d_in[1];
  const unsigned short* wphi = (const unsigned short*)d_in[2];
  const unsigned short* wo = (const unsigned short*)d_in[3];
  const unsigned short* w1 = (const unsigned short*)d_in[5];
  const unsigned short* b1 = (const unsigned short*)d_in[6];
  const unsigned short* w2 = (const unsigned short*)d_in[7];
  const unsigned short* b2 = (const unsigned short*)d_in[8];
  char* ws = (char*)d_ws;
  (void)hipFuncSetAttribute((const void*)k1, hipFuncAttributeMaxDynamicSharedMemorySize, K1_LDS);
  k1<<<256, 256, K1_LDS, stream>>>(x, wqkv, wphi, wo, w1, w2, ws);
  kred<<<34, 256, 0, stream>>>(ws);
  k2a<<<256, 256, 32768, stream>>>(x, ws);
  k2b<<<256, 256, 0, stream>>>(b1, ws);
  k2c<<<256, 256, 0, stream>>>(b2, ws, (unsigned short*)d_out);
}
```

```cpp
#include <hip/hip_runtime.h>
#include <hip/hip_bf16.h>
#include <stdint.h>

#define GAS __attribute__((address_space(1)))
#define LAS __attribute__((address_space(3)))
#define DI __device__ __forceinline__
typedef unsigned v4u __attribute__((ext_vector_type(4)));
typedef unsigned v2u __attribute__((ext_vector_type(2)));
typedef float f32x16 __attribute__((ext_vector_type(16)));
typedef float f32x4 __attribute__((ext_vector_type(4)));
typedef float f32x2 __attribute__((ext_vector_type(2)));
typedef __bf16 bf16x8 __attribute__((ext_vector_type(8)));
typedef __bf16 bf16x2 __attribute__((ext_vector_type(2)));
typedef short s16x4 __attribute__((ext_vector_type(4)));

#define STAMP_DECL
#define STAMP(i)
#define STAMP_FLUSH(k)
namespace {
constexpr int S_ = 2048, D_ = 512, F_ = 1376;
constexpr size_t WOT_OFF = 0;
constexpr size_t W1T_OFF = 512u << 10;
constexpr size_t W2T_OFF = 2048u << 10;
constexpr size_t PHIQ_OFF = 4u << 20;
constexpr size_t KVP_OFF = 8u << 20;
constexpr size_t KVF_OFF = (9u << 20) + (512u << 10);
constexpr size_t HX_OFF = 10u << 20;
constexpr size_t H1X_OFF = 12u << 20;

DI f32x16 mfma(v4u a, v4u b, f32x16 c) {
  return __builtin_amdgcn_mfma_f32_32x32x16_bf16(__builtin_bit_cast(bf16x8, a), __builtin_bit_cast(bf16x8, b), c, 0, 0, 0);
}
DI unsigned pk2(float a, float b) {
  bf16x2 v; v[0] = (__bf16)a; v[1] = (__bf16)b;
  return __builtin_bit_cast(unsigned, v);
}
DI float blo(unsigned v) { return __uint_as_float(v << 16); }
DI float bhi(unsigned v) { return __uint_as_float(v & 0xffff0000u); }
DI v4u pack8(const f32x16& x, int s) {
  v4u p;
  p[0] = pk2(x[8 * s + 0], x[8 * s + 1]); p[1] = pk2(x[8 * s + 2], x[8 * s + 3]);
  p[2] = pk2(x[8 * s + 4], x[8 * s + 5]); p[3] = pk2(x[8 * s + 6], x[8 * s + 7]);
  return p;
}
DI float xsum(float v) { auto r = __builtin_amdgcn_permlane32_swap(__float_as_uint(v), __float_as_uint(v), false, false); return __uint_as_float(r[0]) + __uint_as_float(r[1]); }
DI float xmax(float v) { auto r = __builtin_amdgcn_permlane32_swap(__float_as_uint(v), __float_as_uint(v), false, false); return fmaxf(__uint_as_float(r[0]), __uint_as_float(r[1])); }
DI v2u trread(LAS char* p) { return __builtin_bit_cast(v2u, __builtin_amdgcn_ds_read_tr16_b64_v4i16((LAS s16x4*)p)); }
DI v4u frag_nat(LAS char* blk, int lane) {
  const int hh = lane >> 5, bl = (lane >> 4) & 1, q = (lane & 15) >> 2, p = lane & 3;
  LAS char* a = blk + (8 * hh + q) * 64 + bl * 32 + p * 8;
  v2u lo = trread(a), hi = trread(a + 256);
  return v4u{lo[0], lo[1], hi[0], hi[1]};
}
DI v4u frag_perm(LAS char* blk, int lane) {
  const int hh = lane >> 5, bl = (lane >> 4) & 1, q = (lane & 15) >> 2, p = lane & 3;
  LAS char* a = blk + (4 * hh + q) * 64 + bl * 32 + p * 8;
  v2u lo = trread(a), hi = trread(a + 512);
  return v4u{lo[0], lo[1], hi[0], hi[1]};
}
DI f32x16 zero16() { f32x16 z; for (int i = 0; i < 16; ++i) z[i] = 0.f; return z; }
DI float diag16(const f32x16& d, int lane) {
  const int r = lane & 31, hh = lane >> 5;
  const int istar = (r & 3) + 4 * (r >> 3);
  float v = 0.f;
#pragma unroll
  for (int i = 0; i < 16; ++i) v = (i == istar) ? d[i] : v;
  v = (hh == ((r >> 2) & 1)) ? v : 0.f;
  return xsum(v);
}

DI void dual_softmax(f32x16& p) {
#pragma unroll
  for (int hf = 0; hf < 2; ++hf) {
    float m = p[8 * hf];
#pragma unroll
    for (int i = 1; i < 8; ++i) m = fmaxf(m, p[8 * hf + i]);
    m = xmax(m);
    float s = 0.f;
#pragma unroll
    for (int i = 0; i < 8; ++i) { float e = __expf(p[8 * hf + i] - m); p[8 * hf + i] = e; s += e; }
    s = xsum(s);
    const float inv = 1.0f / s;
#pragma unroll
    for (int i = 0; i < 8; ++i) p[8 * hf + i] *= inv;
  }
}

constexpr int K1_WL = 0;
constexpr int K1_PK = 0;
constexpr int K1_V = 16384;
constexpr int K1_WV = 98304;
constexpr int K1_CS = 147456;
constexpr int K1_MS = 150528;
constexpr int K1_C1 = 154624;
constexpr int K1_LDS = 156672;
constexpr size_t CS1_OFF = (9u << 20) + (640u << 10);
constexpr size_t WPF_OFF = (9u << 20) + (704u << 10);
constexpr size_t DUMMY_OFF = (9u << 20) + (768u << 10);
constexpr size_t FLG_OFF_K1 = (9u << 20) + (832u << 10);
constexpr size_t QF_OFF = 4u << 20;

struct BT { static constexpr bool value = true; };
struct BF { static constexpr bool value = false; };
#define DPP_ADD(v, ctrl) ((v) + __builtin_bit_cast(float, __builtin_amdgcn_update_dpp(0, __builtin_bit_cast(int, (v)), (ctrl), 0xf, 0xf, false)))
DI float dot2(unsigned a, unsigned b, float c) { return __builtin_amdgcn_fdot2_f32_bf16(__builtin_bit_cast(bf16x2, a), __builtin_bit_cast(bf16x2, b), c, false); }
DI void glds3(unsigned voff, const void* sbase, unsigned lds_dst) {
  unsigned keep;
  asm volatile("s_mov_b32 %0, m0\n\ts_mov_b32 m0, %3\n\ts_nop 0\n\tglobal_load_lds_dwordx4 %1, %2\n\tglobal_load_lds_dwordx4 %1, %2 offset:1024\n\t"
               "global_load_lds_dwordx4 %1, %2 offset:2048\n\ts_mov_b32 m0, %0" : "=&s"(keep) : "v"(voff), "s"(sbase), "s"(lds_dst) : "memory");
}
DI void glds1(unsigned voff, const void* sbase, unsigned lds_dst) {
  unsigned keep;
  asm volatile("s_mov_b32 %0, m0\n\ts_mov_b32 m0, %3\n\ts_nop 0\n\tglobal_load_lds_dwordx4 %1, %2\n\ts_mov_b32 m0, %0" : "=&s"(keep) : "v"(voff), "s"(sbase), "s"(lds_dst) : "memory");
}
DI void split3(float x, unsigned& h, unsigned& m, unsigned& l) {
  h = pk2(x, 0.f); const float r1 = x - blo(h);
  m = pk2(r1, 0.f); const float r2 = r1 - blo(m);
  l = pk2(r2, 0.f);
}
DI float vmax(float a, float b) { return __builtin_elementwise_maximum(a, b); }
DI void dual_softmax_sc(f32x16& p, float sc) {
#pragma unroll
  for (int hf = 0; hf < 2; ++hf) {
    float m = vmax(vmax(vmax(p[8 * hf], p[8 * hf + 1]), vmax(p[8 * hf + 2], p[8 * hf + 3])), vmax(vmax(p[8 * hf + 4], p[8 * hf + 5]), vmax(p[8 * hf + 6], p[8 * hf + 7])));
    { auto rr = __builtin_amdgcn_permlane32_swap(__float_as_uint(m), __float_as_uint(m), false, false); m = vmax(__uint_as_float(rr[0]), __uint_as_float(rr[1])); }
    m = -(m * sc);
    float s = 0.f;
#pragma unroll
    for (int i = 0; i < 8; ++i) { float e = __builtin_amdgcn_exp2f(__builtin_fmaf(p[8 * hf + i], sc, m)); p[8 * hf + i] = e; s += e; }
    s = xsum(s);
    const float inv = __builtin_amdgcn_rcpf(s);
#pragma unroll
    for (int i = 0; i < 8; ++i) p[8 * hf + i] *= inv;
  }
}

template <int KH>
DI void k1_body(const unsigned short* __restrict__ x, const unsigned short* __restrict__ wqkv, const unsigned short* __restrict__ wphi,
                const unsigned short* __restrict__ wo, const unsigned short* __restrict__ w1, const unsigned short* __restrict__ w2,
                char* __restrict__ ws, LAS char* sm, unsigned smb, int lane, int tg) {
  const int wv = tg + 4 * KH;
  const int r = lane & 31, hh = lane >> 5;
  const int b = blockIdx.x, xcd = b & 7, bi = b >> 3;
  const int tt = 4 * (xcd >> 1) + (bi & 3), g = 8 * (xcd & 1) + (bi >> 2);
  const int row8 = lane >> 3, part = lane & 7, p4 = lane & 3;
  STAMP_DECL; STAMP(0);
  unsigned we[8];
  v4u xr[16];
  const unsigned xvo = row8 * 1024 + part * 16;
  const char* xsb = (const char*)x + (size_t)(tt * 128 + tg * 32) * 1024 + KH * 512;
  const unsigned wvo = (lane >> 2) * 3072 + p4 * 16;
  const char* wsb = (const char*)wqkv + (size_t)(256 * KH + 16 * tg) * 3072 + g * 64;
  const unsigned wld = smb + K1_WL + ((16 * KH + tg) * 3) * 1024;
#define XLOAD(R) _Pragma("unroll") for (int rg = 0; rg < 4; ++rg) \
    asm volatile("global_load_dwordx4 %0, %1, %2 offset:%3" : "=v"(xr[4 * (R) + rg]) : "v"(xvo), "s"(xsb + rg * 8192), "n"((R) * 128) : "memory")
#define WLOAD(R) glds3(wvo, wsb + (size_t)(R) * 64 * 3072, wld + (R) * 12288)
#define XLOAD1(R, rg) asm volatile("global_load_dwordx4 %0, %1, %2 offset:%3" : "=v"(xr[4 * (R) + (rg)]) : "v"(xvo), "s"(xsb + (rg) * 8192), "n"((R) * 128) : "memory")
  XLOAD(0); WLOAD(0);
  XLOAD(1);
  size_t rt_dst[2];
  const bool isw1 = b < 86;
  STAMP(1);
  f32x16 aq = zero16(), ak = zero16(), av = zero16(), c0 = zero16(), c1 = zero16(), c2 = zero16();
  const v4u ones = v4u{0x3f803f80u, 0x3f803f80u, 0x3f803f80u, 0x3f803f80u};
  float sa = 0.f, sb = 0.f, qa = 0.f, qb = 0.f;
  const int bl = (lane >> 4) & 1, q4 = (lane & 15) >> 2;
  LAS char* ap0 = sm + K1_WL + (16 * KH) * 3072 + (8 * hh + q4) * 64 + bl * 32 + p4 * 8;
  LAS char* ap[4] = {ap0 + tg * 3072, ap0 + (tg ^ 1) * 3072, ap0 + (tg ^ 2) * 3072, ap0 + (tg ^ 3) * 3072};
  LAS char* stg = sm + K1_WV + wv * 6144;
  LAS char* stw0 = stg + row8 * 128 + ((part ^ (row8 >> 1)) << 4);
  LAS char* stw1 = stg + row8 * 128 + ((part ^ (row8 >> 1) ^ 4) << 4);
  const unsigned srd = (r * 128 + ((hh ^ ((r >> 1) & 7)) << 4)) ^ (tg << 5);
#define SB() __builtin_amdgcn_sched_barrier(0)
#define XWAIT(R, N) asm volatile("s_waitcnt vmcnt(%4)" : "+v"(xr[4 * (R)]), "+v"(xr[4 * (R) + 1]), "+v"(xr[4 * (R) + 2]), "+v"(xr[4 * (R) + 3]) : "n"(N) : "memory")
#define STAGE(R, XF) do { *(LAS v4u*)(stw0) = xr[4 * (R)]; *(LAS v4u*)(stw1 + 1024) = xr[4 * (R) + 1]; *(LAS v4u*)(stw0 + 2048) = xr[4 * (R) + 2]; \
    *(LAS v4u*)(stw1 + 3072) = xr[4 * (R) + 3]; _Pragma("unroll") for (int u_ = 0; u_ < 4; ++u_) XF[u_] = *(LAS v4u*)(stg + (srd ^ (u_ << 5))); } while (0)
#define STAT(v) do { const float lo_ = blo(v), hi_ = bhi(v); sa += lo_; sb += hi_; qa = __builtin_fmaf(lo_, lo_, qa); qb = __builtin_fmaf(hi_, hi_, qb); asm volatile("" : "+v"(sa), "+v"(sb), "+v"(qa), "+v"(qb)); } while (0)
  v4u xf[2][4];
  XWAIT(0, 7);
  STAGE(0, xf[0]);
  asm volatile("s_waitcnt vmcnt(4)" ::: "memory");
#pragma unroll
  for (int rho = 0; rho < 4; ++rho) {
    __builtin_amdgcn_s_barrier();
    STAMP(2 + rho);
    v4u f[2][3];
#pragma unroll
    for (int c = 0; c < 3; ++c) { v2u l = trread(ap[0] + rho * 12288 + c * 1024), h = trread(ap[0] + rho * 12288 + c * 1024 + 256); f[0][c] = v4u{l[0], l[1], h[0], h[1]}; }
    SB();
#pragma unroll
    for (int u = 0; u < 4; ++u) {
      const v4u xb = xf[rho & 1][u];
#pragma unroll
      for (int c = 0; c < 3; ++c) {
        if (c == 0) aq = mfma(f[u & 1][0], xb, aq);
        if (c == 1) ak = mfma(f[u & 1][1], xb, ak);
        if (c == 2) av = mfma(f[u & 1][2], xb, av);
        if (u < 3) { LAS char* a = ap[u + 1] + rho * 12288 + c * 1024; v2u l = trread(a), h = trread(a + 256); f[(u + 1) & 1][c] = v4u{l[0], l[1], h[0], h[1]}; }
        STAT(xb[c]);
        if (rho < 3 && u == 0 && c == 1) WLOAD(rho + 1);
        if (rho < 2 && u >= 1 && u <= 2 && c != 1) XLOAD1(rho + 2, (u - 1) * 2 + (c >> 1));
        if (rho == 2 && u == 2 && c == 0) {
#pragma unroll
      for (int u2 = 0; u2 < 2; ++u2) {
        const int i = 2 * wv + u2;
        const char* src; size_t dst;
        if (isw1) {
          const int T = b >> 1, s = 16 * (b & 1) + i;
          const int e = T - 32;
          const int q = T < 32 ? (T & 15) : (4 * (e & 3) + (e >> 2)), ti = T < 32 ? (T >> 4) : 2;
          src = (const char*)w1 + (size_t)s * 44032 + T * 64;
          dst = W1T_OFF + (size_t)((q * 32 + s) * 3 + ti) * 1024;
        } else {
          int id = (b - 86) * 12 + i;
          const bool ok = (i < 12) && (id < 1888);
          id = ok ? id : 0;
          const int sb = id >> 4, T = id & 15;
          src = (sb < 32 ? (const char*)wo + sb * 16384 : (const char*)w2 + (sb - 32) * 16384) + T * 64;
          dst = sb < 32 ? WOT_OFF + (size_t)(T * 32 + sb) * 1024 : W2T_OFF + (size_t)(T * 86 + sb - 32) * 1024;
          if (!ok) dst = DUMMY_OFF;
        }
        rt_dst[u2] = dst;
        glds1((lane >> 2) * (isw1 ? 2752 : 1024) + p4 * 16, src, smb + K1_WV + wv * 6144 + 4096 + u2 * 1024);
      }
    }
        SB();
        if (u == 0) {
          if (c == 0) c0 = mfma(ones, f[0][0], c0);
          if (c == 1) c1 = mfma(ones, f[0][1], c1);
          if (c == 2) c2 = mfma(ones, f[0][2], c2);
          if (c == 2) STAT(xb[3]);
          SB();
        }
      }
      if (u > 0) { STAT(xb[3]); SB(); }
    }
    if (rho < 3) {
      XWAIT(rho + 1, rho == 2 ? 5 : 7);
      STAGE(rho + 1, xf[(rho + 1) & 1]);
      asm volatile("s_waitcnt vmcnt(%0)" :: "n"(rho == 2 ? 2 : 4) : "memory");
    }
    if (rho == 3) {
      asm volatile("s_waitcnt vmcnt(0)" ::: "memory");
      v4u f0 = frag_perm(stg + 4096, lane), f1 = frag_perm(stg + 5120, lane);
      *((GAS v4u*)(ws + rt_dst[0]) + lane) = f0;
      *((GAS v4u*)(ws + rt_dst[1]) + lane) = f1;
#pragma unroll
      for (int j = 0; j < 8; ++j) {
        const int d = 8 * (j >> 2) + 4 * hh + (j & 3);
        we[j] = wphi[((2 * g + KH) * 16 + d) * 16 + (r & 15)];
      }
      if (isw1) {
        f32x16 c = mfma(ones, f0, zero16());
        c = mfma(ones, f1, c);
        if (hh == 0) ((LAS float*)(sm + K1_C1))[wv * 32 + r] = c[0];
      }
    }
  }
  STAMP(6);
  *(LAS f32x2*)(sm + K1_MS + (tg * 32 + r) * 32 + KH * 16 + hh * 8) = f32x2{sa + sb, qa + qb};
  if (hh == 0) { LAS float* cs = (LAS float*)(sm + K1_CS) + r * 8 + wv; cs[0] = c0[0]; cs[256] = c1[0]; cs[512] = c2[0]; }
  {
    constexpr int O = 8 * (1 - KH);
    LAS f32x4* ex = (LAS f32x4*)(stg) + lane;
    ex[0] = f32x4{aq[O], aq[O + 1], aq[O + 2], aq[O + 3]}; ex[64] = f32x4{aq[O + 4], aq[O + 5], aq[O + 6], aq[O + 7]};
    ex[128] = f32x4{ak[O], ak[O + 1], ak[O + 2], ak[O + 3]}; ex[192] = f32x4{ak[O + 4], ak[O + 5], ak[O + 6], ak[O + 7]};
    ex[256] = f32x4{av[O], av[O + 1], av[O + 2], av[O + 3]}; ex[320] = f32x4{av[O + 4], av[O + 5], av[O + 6], av[O + 7]};
  }
  asm volatile("s_waitcnt lgkmcnt(0)" ::: "memory");
  __builtin_amdgcn_s_barrier();
  STAMP(7);
  constexpr int M = 8 * KH;
  float rstd;
  {
    LAS f32x4* ex = (LAS f32x4*)(sm + K1_WV + (wv ^ 4) * 6144) + lane;
    const f32x4 e0 = ex[0], e1 = ex[64], e2 = ex[128], e3 = ex[192], e4 = ex[256], e5 = ex[320];
    LAS f32x4* sp = (LAS f32x4*)(sm + K1_MS + (tg * 32 + r) * 32);
    const f32x4 st = sp[0] + sp[1];
    LAS f32x4* cp = (LAS f32x4*)(sm + K1_CS + r * 32);
    const f32x4 ca = cp[0] + cp[1], cb = cp[64] + cp[65], cc = cp[128] + cp[129];
    float cs0 = (ca[0] + ca[1]) + (ca[2] + ca[3]), cs1 = (cb[0] + cb[1]) + (cb[2] + cb[3]), cs2 = (cc[0] + cc[1]) + (cc[2] + cc[3]);
    if (hh) { cs0 = 0.f; cs1 = 0.f; cs2 = 0.f; }
#pragma unroll
    for (int e = 0; e < 4; ++e) {
      aq[M + e] += e0[e]; aq[M + 4 + e] += e1[e];
      ak[M + e] += e2[e]; ak[M + 4 + e] += e3[e];
      av[M + e] += e4[e]; av[M + 4 + e] += e5[e];
    }
    const float mean = (st[0] + st[2]) * (1.0f / 512.0f);
    const float var = fmaxf((st[1] + st[3]) * (1.0f / 512.0f) - mean * mean, 0.f);
    rstd = __builtin_amdgcn_rsqf(var + 1e-5f);
    ak = __builtin_amdgcn_mfma_f32_32x32x2f32(cs1, -mean, ak, 0, 0, 0);
    aq = __builtin_amdgcn_mfma_f32_32x32x2f32(cs0, -mean, aq, 0, 0, 0);
    av = __builtin_amdgcn_mfma_f32_32x32x2f32(cs2, -mean, av, 0, 0, 0);
  }
  const unsigned sg = (r >= 16) ? 0x80008000u : 0u;
  const v4u wp = v4u{(we[0] | (we[1] << 16)) ^ sg, (we[2] | (we[3] << 16)) ^ sg, (we[4] | (we[5] << 16)) ^ sg, (we[6] | (we[7] << 16)) ^ sg};
  f32x16 pk = mfma(wp, pack8(ak, KH), zero16());
  {
    LAS char* vi = sm + K1_V + tg * 2048 + r * 64 + KH * 32 + hh * 8;
    *(LAS v2u*)(vi) = v2u{pk2(av[M] * rstd, av[M + 1] * rstd), pk2(av[M + 2] * rstd, av[M + 3] * rstd)};
    *(LAS v2u*)(vi + 16) = v2u{pk2(av[M + 4] * rstd, av[M + 5] * rstd), pk2(av[M + 6] * rstd, av[M + 7] * rstd)};
#pragma unroll
    for (int i = 0; i < 8; ++i) aq[M + i] *= rstd;
    *((GAS v4u*)(ws + QF_OFF + (size_t)((tt * 4 + tg) * 32 + 2 * g + KH) * 1024) + lane) = pack8(aq, KH);
    if (tt == 0 && tg == 0) *((GAS v4u*)(ws + WPF_OFF + (2 * g + KH) * 1024) + lane) = wp;
  }
  dual_softmax_sc(pk, rstd * 1.4426950408889634f);
  {
    LAS char* pi = sm + K1_PK + (tg * 2 + KH) * 2048 + r * 64 + hh * 8;
#pragma unroll
    for (int gq = 0; gq < 4; ++gq) *(LAS v2u*)(pi + gq * 16) = v2u{pk2(pk[4 * gq], pk[4 * gq + 1]), pk2(pk[4 * gq + 2], pk[4 * gq + 3])};
  }
  asm volatile("s_waitcnt lgkmcnt(0)" ::: "memory");
  __builtin_amdgcn_s_barrier();
  STAMP(8);
  if (tg == 0) {
    f32x16 z = zero16();
    const bool isv = (KH == 0) ? (r < 16) : (r >= 16);
#pragma unroll
    for (int ks = 0; ks < 8; ++ks) {
      v4u a = frag_nat(sm + K1_PK + ((ks >> 1) * 2 + KH) * 2048 + (ks & 1) * 1024, lane);
      v4u bv = frag_nat(sm + K1_V + (ks >> 1) * 2048 + (ks & 1) * 1024, lane);
      if (!isv) bv = ones;
      z = mfma(a, bv, z);
    }
    const int row = isv ? (r & 15) : 16;
    if (isv || ((r & 15) == 0)) {
      GAS f32x4* dst = (GAS f32x4*)(ws + KVP_OFF + (size_t)(tt * 32 + 2 * g + KH) * 2176 + row * 128 + hh * 64);
#pragma unroll
      for (int gq = 0; gq < 4; ++gq) dst[gq] = f32x4{z[4 * gq], z[4 * gq + 1], z[4 * gq + 2], z[4 * gq + 3]};
    }
  } else if (KH == 1 && tg == 2) {
    if (b < 136) ((GAS unsigned*)(ws + KVF_OFF))[b * 64 + lane] = 0xFFFFFFFFu;
    if (lane < 16) ((GAS unsigned*)(ws + FLG_OFF_K1))[(b * 16 + lane) * 16] = 0u;
  } else if (KH == 0 && tg == 1 && isw1 && lane < 32) {
    LAS float* cp = (LAS float*)(sm + K1_C1) + lane;
    float s = 0.f;
#pragma unroll
    for (int k = 0; k < 8; ++k) s += cp[32 * k];
    ((GAS float*)(ws + CS1_OFF))[b * 32 + lane] = s;
  }
  STAMP(9); STAMP_FLUSH(0);
}

__global__ __launch_bounds__(512, 2) void k1(const unsigned short* __restrict__ x, const unsigned short* __restrict__ wqkv,
                                             const unsigned short* __restrict__ wphi, const unsigned short* __restrict__ wo,
                                             const unsigned short* __restrict__ w1, const unsigned short* __restrict__ w2, char* __restrict__ ws) {
  extern __shared__ __attribute__((aligned(16))) char sm_[];
  const int tid = threadIdx.x, lane = tid & 63, wv = __builtin_amdgcn_readfirstlane(tid >> 6);
  if (wv >> 2) k1_body<1>(x, wqkv, wphi, wo, w1, w2, ws, (LAS char*)sm_, (unsigned)(size_t)sm_, lane, wv & 3);
  else k1_body<0>(x, wqkv, wphi, wo, w1, w2, ws, (LAS char*)sm_, (unsigned)(size_t)sm_, lane, wv & 3);
}

constexpr size_t FLG_OFF = 18u << 20;
constexpr int K2_OF = 0;
constexpr int K2_HF = 32768;
constexpr int K2_H1 = 65536;
constexpr int K2_ST = 153600;
constexpr int K2_LDS = 155648;
constexpr int RD = 28;
constexpr unsigned WS_BYTES = 18u << 20;
typedef GAS unsigned gu32;
#define RLX_AGENT __ATOMIC_RELAXED, __HIP_MEMORY_SCOPE_AGENT

DI void wait_flags(gu32* f, int j, int lane) {
  const bool need = lane < 4 && lane != j;
  for (unsigned sp = 0; sp < (1u << 22); ++sp) {
    unsigned v = 1u;
    if (need) v = __hip_atomic_load(f + lane * 16, RLX_AGENT);
    if (__builtin_amdgcn_ballot_w64(v == 0u) == 0ull) break;
    __builtin_amdgcn_s_sleep(1);
  }
  __builtin_amdgcn_fence(__ATOMIC_ACQUIRE, "wavefront");
}

template <int KH, int NT>
DI void k2_body(const unsigned short* __restrict__ x, const unsigned short* __restrict__ b1, const unsigned short* __restrict__ b2,
                char* __restrict__ ws, unsigned short* __restrict__ out, LAS char* sm, int lane, int w) {
  const int wv = w + 4 * KH;
  const int r = lane & 31, hh = lane >> 5;
  const int b = blockIdx.x, c = b >> 2, j = b & 3, q = 4 * j + w;
  constexpr int M = 8 * KH, O = 8 * (1 - KH);
  constexpr int NA = 16, NB = 16 * NT, NC = 43, NTOT = NA + NB + NC;
  const __amdgpu_buffer_rsrc_t rs = __builtin_amdgcn_make_buffer_rsrc((void*)ws, 0, (int)WS_BYTES, 0x00020000);
  const int l16 = lane * 16;
  STAMP_DECL; STAMP(0);
  if (KH == 1 && w == 3 && b < 136) {
    f32x2 p[16];
#pragma unroll
    for (int t = 0; t < 16; ++t) p[t] = *((const GAS f32x2*)(ws + KVP_OFF + (size_t)t * 69632) + b * 64 + lane);
    f32x2 s = p[0];
#pragma unroll
    for (int t = 1; t < 16; ++t) s += p[t];
    unsigned v = pk2(s[0], s[1]);
    v = (v == 0xFFFFFFFFu) ? 0xFFFEFFFEu : v;
    __hip_atomic_store((gu32*)(ws + KVF_OFF) + b * 64 + lane, v, RLX_AGENT);
  }
  v4u qf[4], wpf[4];
#pragma unroll
  for (int hq = 0; hq < 4; ++hq) {
    qf[hq] = __builtin_amdgcn_raw_buffer_load_b128(rs, l16, (int)QF_OFF + (c * 32 + 4 * wv + hq) * 1024, 0);
    wpf[hq] = __builtin_amdgcn_raw_buffer_load_b128(rs, l16, (int)WPF_OFF + (4 * wv + hq) * 1024, 0);
  }
  const int n0 = 128 * j + 32 * w + 16 * KH + 4 * hh;
  const v2u xa = *(const GAS v2u*)((const char*)x + (size_t)(32 * c + r) * 1024 + n0 * 2);
  const v2u xb2 = *(const GAS v2u*)((const char*)x + (size_t)(32 * c + r) * 1024 + n0 * 2 + 16);
  float ab1[NT];
#pragma unroll
  for (int t = 0; t < NT; ++t) {
    const int T = (t < 2) ? (q + 16 * t) : (32 + 4 * w + j);
    const GAS float* cp = (const GAS float*)(ws + CS1_OFF) + T * 64 + r;
    const float cs = cp[0] + cp[32];
    const float bb = blo(b1[32 * T + r]);
    ab1[t] = hh ? bb : cs;
  }
  const float ab2 = hh ? 0.f : blo(b2[128 * j + 32 * w + r]);
  v4u ring[RD];
  const int sA = (int)WOT_OFF + (q * 32 + 16 * KH) * 1024, sB = (int)W1T_OFF + ((q * 32 + 16 * KH) * 3) * 1024, sC = (int)W2T_OFF + (q * 86 + 43 * KH) * 1024;
#define RLOAD(i) do { if ((i) < NTOT) ring[(i) % RD] = __builtin_amdgcn_raw_buffer_load_b128(rs, l16, (i) < NA ? sA + (i) * 1024 : (i) < NA + NB ? \
    sB + ((((i) - NA) / NT) * 3 + ((i) - NA) % NT) * 1024 : sC + ((i) - NA - NB) * 1024, 0); } while (0)
#define RLOADS(lo, hi) _Pragma("unroll") for (int i_ = (lo); i_ < (hi); ++i_) RLOAD(i_)
  RLOADS(0, RD);
  SB();
  STAMP(1);
  v4u pf[4][2];
#pragma unroll
  for (int hq = 0; hq < 4; ++hq) {
    f32x16 pq = mfma(wpf[hq], qf[hq], zero16());
    dual_softmax_sc(pq, 1.4426950408889634f);
    pf[hq][0] = pack8(pq, 0); pf[hq][1] = pack8(pq, 1);
  }
  STAMP(2);
  v4u kv[4][2];
  {
    const int ko = (r < 16 ? r : 16) * 64 + hh * 32;
    for (unsigned sp = 0; sp < (1u << 22); ++sp) {
      unsigned bad = 0;
#pragma unroll
      for (int hq = 0; hq < 4; ++hq)
#pragma unroll
        for (int s = 0; s < 2; ++s) {
          kv[hq][s] = __builtin_amdgcn_raw_buffer_load_b128(rs, ko + s * 16, (int)KVF_OFF + (4 * wv + hq) * 1088, 16);
          bad |= (kv[hq][s][0] == 0xFFFFFFFFu) | (kv[hq][s][1] == 0xFFFFFFFFu) | (kv[hq][s][2] == 0xFFFFFFFFu) | (kv[hq][s][3] == 0xFFFFFFFFu);
        }
      if (__builtin_amdgcn_ballot_w64(bad != 0) == 0ull) break;
      __builtin_amdgcn_s_sleep(2);
    }
  }
  STAMP(3);
#pragma unroll
  for (int hq = 0; hq < 4; ++hq) {
    f32x16 d = mfma(kv[hq][0], pf[hq][0], zero16());
    d = mfma(kv[hq][1], pf[hq][1], d);
    const float inv = __builtin_amdgcn_rcpf(d[8] + 1e-6f);
#pragma unroll
    for (int i = 0; i < 8; ++i) d[i] *= inv;
    *(LAS v4u*)(sm + K2_OF + (4 * wv + hq) * 1024 + l16) = pack8(d, 0);
  }
  asm volatile("s_waitcnt lgkmcnt(0)" ::: "memory");
  __builtin_amdgcn_s_barrier();
  STAMP(4);
  v4u hfrag;
  {
    f32x16 acc = zero16();
    acc[M + 0] = blo(xa[0]); acc[M + 1] = bhi(xa[0]); acc[M + 2] = blo(xa[1]); acc[M + 3] = bhi(xa[1]);
    acc[M + 4] = blo(xb2[0]); acc[M + 5] = bhi(xb2[0]); acc[M + 6] = blo(xb2[1]); acc[M + 7] = bhi(xb2[1]);
#pragma unroll
    for (int i = 0; i < NA; ++i) acc = mfma(ring[i % RD], *(LAS v4u*)(sm + K2_OF + (16 * KH + i) * 1024 + l16), acc);
    LAS f32x4* ex = (LAS f32x4*)(sm + K2_H1 + wv * 2048) + lane;
    ex[0] = f32x4{acc[O], acc[O + 1], acc[O + 2], acc[O + 3]}; ex[64] = f32x4{acc[O + 4], acc[O + 5], acc[O + 6], acc[O + 7]};
    asm volatile("s_waitcnt lgkmcnt(0)" ::: "memory");
    __builtin_amdgcn_s_barrier();
    LAS f32x4* ey = (LAS f32x4*)(sm + K2_H1 + (wv ^ 4) * 2048) + lane;
    const f32x4 e0 = ey[0], e1 = ey[64];
#pragma unroll
    for (int e = 0; e < 4; ++e) { acc[M + e] += e0[e]; acc[M + 4 + e] += e1[e]; }
    hfrag = pack8(acc, KH);
  }
  __builtin_amdgcn_raw_buffer_store_b128(hfrag, rs, l16, (int)HX_OFF + (c * 32 + 2 * q + KH) * 1024, 16);
  *(LAS v4u*)(sm + K2_HF + (2 * q + KH) * 1024 + l16) = hfrag;
  gu32* fl0 = (gu32*)(ws + FLG_OFF) + ((c * 8 + wv) * 4) * 16;
  asm volatile("s_waitcnt vmcnt(0)" ::: "memory");
  if (lane == 0) __hip_atomic_store(fl0 + j * 16, 1u, RLX_AGENT);
  STAMP(5);
  wait_flags(fl0, j, lane);
  {
    v4u g[3];
#pragma unroll
    for (int p = 0; p < 3; ++p) {
      const int jp = (j + 1 + p) & 3;
      g[p] = __builtin_amdgcn_raw_buffer_load_b128(rs, l16, (int)HX_OFF + (c * 32 + 2 * (4 * jp + w) + KH) * 1024, 16);
    }
    RLOADS(RD, NA + RD);
    SB();
#pragma unroll
    for (int p = 0; p < 3; ++p) {
      const int jp = (j + 1 + p) & 3;
      *(LAS v4u*)(sm + K2_HF + (2 * (4 * jp + w) + KH) * 1024 + l16) = g[p];
    }
  }
  asm volatile("s_waitcnt lgkmcnt(0)" ::: "memory");
  __builtin_amdgcn_s_barrier();
  STAMP(6);
  {
    const v4u ones = v4u{0x3f803f80u, 0x3f803f80u, 0x3f803f80u, 0x3f803f80u};
    f32x16 ag = zero16(), as = zero16();
#pragma unroll
    for (int u = 0; u < 4; ++u) {
      const v4u hb = *(LAS v4u*)(sm + K2_HF + (4 * wv + u) * 1024 + l16);
      ag = mfma(hb, hb, ag);
      as = mfma(ones, hb, as);
    }
    const float dg = diag16(ag, lane);
    if (hh == 0) *(LAS f32x2*)(sm + K2_ST + r * 64 + wv * 8) = f32x2{as[0], dg};
  }
  f32x16 ac[NT];
#pragma unroll
  for (int t = 0; t < NT; ++t) ac[t] = zero16();
  constexpr int QUIET = 4;
#pragma unroll
  for (int s = 0; s < 16; ++s) {
    const v4u hb = *(LAS v4u*)(sm + K2_HF + (16 * KH + s) * 1024 + l16);
#pragma unroll
    for (int t = 0; t < NT; ++t) {
      const int i = NA + s * NT + t;
      ac[t] = mfma(ring[i % RD], hb, ac[t]);
      if (s < 16 - QUIET) RLOAD(i + RD);
      SB();
    }
  }
  STAMP(7);
  {
    LAS char* e01 = sm + K2_OF + wv * 4096 + l16;
    LAS char* e2 = sm + K2_H1 + (((j + 1 + KH) & 3) * 8) * 1024 + w * 2048 + l16;
#pragma unroll
    for (int t = 0; t < NT; ++t) {
      LAS char* d = t < 2 ? e01 + t * 2048 : e2;
      *(LAS f32x4*)(d) = f32x4{ac[t][O], ac[t][O + 1], ac[t][O + 2], ac[t][O + 3]};
      *(LAS f32x4*)(d + 1024) = f32x4{ac[t][O + 4], ac[t][O + 5], ac[t][O + 6], ac[t][O + 7]};
    }
    asm volatile("s_waitcnt lgkmcnt(0)" ::: "memory");
    __builtin_amdgcn_s_barrier();
    LAS char* f01 = sm + K2_OF + (wv ^ 4) * 4096 + l16;
    LAS char* f2 = sm + K2_H1 + (((j + 2 - KH) & 3) * 8) * 1024 + w * 2048 + l16;
    LAS f32x4* sp = (LAS f32x4*)(sm + K2_ST + r * 64);
    const f32x4 s0 = sp[0] + sp[1], s1 = sp[2] + sp[3];
    const f32x4 st = s0 + s1;
    const float mean = (st[0] + st[2]) * (1.0f / 512.0f);
    const float var = fmaxf((st[1] + st[3]) * (1.0f / 512.0f) - mean * mean, 0.f) + 1e-5f;
    const float rstd = __builtin_amdgcn_rsqf(var);
    const float bop = hh ? var * rstd : -mean;
    const float r2 = rstd * rstd;
#pragma unroll
    for (int t = 0; t < NT; ++t) {
      LAS char* d = t < 2 ? f01 + t * 2048 : f2;
      const f32x4 e0 = *(LAS f32x4*)(d), e1 = *(LAS f32x4*)(d + 1024);
#pragma unroll
      for (int e = 0; e < 4; ++e) { ac[t][M + e] += e0[e]; ac[t][M + 4 + e] += e1[e]; }
      ac[t] = __builtin_amdgcn_mfma_f32_32x32x2f32(ab1[t], bop, ac[t], 0, 0, 0);
    }
    v4u h1f[NT];
#pragma unroll
    for (int t = 0; t < NT; ++t) {
#pragma unroll
      for (int e = 0; e < 8; ++e) { const float p = vmax(ac[t][M + e], 0.f); ac[t][M + e] = (p * p) * r2; }
      h1f[t] = pack8(ac[t], KH);
      const int T = (t < 2) ? (q + 16 * t) : (32 + 4 * w + j);
      __builtin_amdgcn_raw_buffer_store_b128(h1f[t], rs, l16, (int)H1X_OFF + (c * 86 + 2 * T + KH) * 1024, 16);
    }
    asm volatile("s_waitcnt vmcnt(0) lgkmcnt(0)" ::: "memory");
    gu32* fl1 = (gu32*)(ws + FLG_OFF + 131072) + ((c * 8 + wv) * 4) * 16;
    if (lane == 0) __hip_atomic_store(fl1 + j * 16, 1u, RLX_AGENT);
    __builtin_amdgcn_s_barrier();
#pragma unroll
    for (int t = 0; t < NT; ++t) {
      const int T = (t < 2) ? (q + 16 * t) : (32 + 4 * w + j);
      *(LAS v4u*)(sm + K2_H1 + (2 * T + KH) * 1024 + l16) = h1f[t];
    }
    STAMP(8);
    wait_flags(fl1, j, lane);
    v4u g[3][3];
#pragma unroll
    for (int p = 0; p < 3; ++p) {
      const int jp = (j + 1 + p) & 3;
#pragma unroll
      for (int t = 0; t < 3; ++t) {
        const int T = (t < 2) ? (4 * jp + w + 16 * t) : (32 + 4 * w + jp);
        const int Tc = T < 43 ? T : (4 * jp + w);
        g[p][t] = __builtin_amdgcn_raw_buffer_load_b128(rs, l16, (int)H1X_OFF + (c * 86 + 2 * Tc + KH) * 1024, 16);
      }
    }
    RLOADS(NA + (16 - QUIET) * NT + RD, NA + NB + RD);
    SB();
#pragma unroll
    for (int p = 0; p < 3; ++p) {
      const int jp = (j + 1 + p) & 3;
#pragma unroll
      for (int t = 0; t < 3; ++t) {
        const int T = (t < 2) ? (4 * jp + w + 16 * t) : (32 + 4 * w + jp);
        if (T < 43) *(LAS v4u*)(sm + K2_H1 + (2 * T + KH) * 1024 + l16) = g[p][t];
      }
    }
  }
  asm volatile("s_waitcnt lgkmcnt(0)" ::: "memory");
  __builtin_amdgcn_s_barrier();
  STAMP(9);
  {
    f32x16 acc = zero16();
    if (KH == 0) acc = __builtin_amdgcn_mfma_f32_32x32x2f32(ab2, 1.0f, acc, 0, 0, 0);
#pragma unroll
    for (int e = 0; e < 4; ++e) { acc[M + 2 * e] += blo(hfrag[e]); acc[M + 2 * e + 1] += bhi(hfrag[e]); }
#pragma unroll
    for (int s = 0; s < NC; ++s) {
      const int i = NA + NB + s;
      acc = mfma(ring[i % RD], *(LAS v4u*)(sm + K2_H1 + (43 * KH + s) * 1024 + l16), acc);
      RLOAD(i + RD);
      SB();
    }
    STAMP(10);
    LAS f32x4* ex = (LAS f32x4*)(sm + K2_OF + wv * 2048) + lane;
    ex[0] = f32x4{acc[O], acc[O + 1], acc[O + 2], acc[O + 3]}; ex[64] = f32x4{acc[O + 4], acc[O + 5], acc[O + 6], acc[O + 7]};
    asm volatile("s_waitcnt lgkmcnt(0)" ::: "memory");
    __builtin_amdgcn_s_barrier();
    LAS f32x4* ey = (LAS f32x4*)(sm + K2_OF + (wv ^ 4) * 2048) + lane;
    const f32x4 e0 = ey[0], e1 = ey[64];
#pragma unroll
    for (int e = 0; e < 4; ++e) { acc[M + e] += e0[e]; acc[M + 4 + e] += e1[e]; }
    v4u o = pack8(acc, KH);
    auto r0 = __builtin_amdgcn_permlane32_swap(o[0], o[2], false, false);
    auto r1 = __builtin_amdgcn_permlane32_swap(o[1], o[3], false, false);
    *(GAS v4u*)((char*)out + (size_t)(32 * c + r) * 1024 + (128 * j + 32 * w + 16 * KH + 8 * hh) * 2) = v4u{r0[0], r1[0], r0[1], r1[1]};
  }
  STAMP(11); STAMP_FLUSH(1);
}

__global__ __launch_bounds__(512, 2) void k2(const unsigned short* __restrict__ x, const unsigned short* __restrict__ b1,
                                             const unsigned short* __restrict__ b2, char* __restrict__ ws, unsigned short* __restrict__ out) {
  extern __shared__ __attribute__((aligned(16))) char sm_[];
  const int tid = threadIdx.x, lane = tid & 63, wv = __builtin_amdgcn_readfirstlane(tid >> 6);
  const int w = wv & 3, j = blockIdx.x & 3;
  const bool three = (4 * w + j) < 11;
  if (wv >> 2) { if (three) k2_body<1, 3>(x, b1, b2, ws, out, (LAS char*)sm_, lane, w); else k2_body<1, 2>(x, b1, b2, ws, out, (LAS char*)sm_, lane, w); }
  else { if (three) k2_body<0, 3>(x, b1, b2, ws, out, (LAS char*)sm_, lane, w); else k2_body<0, 2>(x, b1, b2, ws, out, (LAS char*)sm_, lane, w); }
}
}

extern "C" void kernel_launch(void* const* d_in, const int* in_sizes, int n_in, void* d_out, int out_size, void* d_ws, size_t ws_size,
                              hipStream_t stream) {
  const unsigned short* x = (const unsigned short*)d_in[0];
  const unsigned short* wqkv = (const unsigned short*)d_in[1];
  const unsigned short* wphi = (const unsigned short*)d_in[2];
  const unsigned short* wo = (const unsigned short*)d_in[3];
  const unsigned short* w1 = (const unsigned short*)d_in[5];
  const unsigned short* b1 = (const unsigned short*)d_in[6];
  const unsigned short* w2 = (const unsigned short*)d_in[7];
  const unsigned short* b2 = (const unsigned short*)d_in[8];
  char* ws = (char*)d_ws;
  (void)hipFuncSetAttribute((const void*)k1, hipFuncAttributeMaxDynamicSharedMemorySize, K1_LDS);
  k1<<<256, 512, K1_LDS, stream>>>(x, wqkv, wphi, wo, w1, w2, ws);
  (void)hipFuncSetAttribute((const void*)k2, hipFuncAttributeMaxDynamicSharedMemorySize, K2_LDS);
  k2<<<256, 512, K2_LDS, stream>>>(x, b1, b2, ws, (unsigned short*)d_out);
}
```

```cpp
#include <hip/hip_runtime.h>
#include <hip/hip_bf16.h>
#include <stdint.h>

#define GAS __attribute__((address_space(1)))
#define LAS __attribute__((address_space(3)))
#define DI __device__ __forceinline__
typedef unsigned v4u __attribute__((ext_vector_type(4)));
typedef unsigned v2u __attribute__((ext_vector_type(2)));
typedef float f32x16 __attribute__((ext_vector_type(16)));
typedef float f32x4 __attribute__((ext_vector_type(4)));
typedef float f32x2 __attribute__((ext_vector_type(2)));
typedef __bf16 bf16x8 __attribute__((ext_vector_type(8)));
typedef __bf16 bf16x2 __attribute__((ext_vector_type(2)));
typedef short s16x4 __attribute__((ext_vector_type(4)));
typedef GAS unsigned gu32;

#define STAMP_DECL
#define STAMP(i)
#define STAMP_FLUSH(k)

namespace {
constexpr int WOT_OFF = 0;
constexpr int W1T_OFF = 512 << 10;
constexpr int W2T_OFF = 2048 << 10;
constexpr int QF_OFF = 16 << 20;
constexpr int KVP_OFF = 6 << 20;
constexpr int KVF_OFF = (7 << 20) + (128 << 10);
constexpr int KVF_REP = 36864;
constexpr int CS1_OFF = (7 << 20) + (640 << 10);
constexpr int WPF_OFF = (7 << 20) + (704 << 10);
constexpr int DUMMY_OFF = (7 << 20) + (768 << 10);
constexpr int HX_OFF = 8 << 20;
constexpr int H1X_OFF = 10 << 20;
constexpr int FLG_OFF = (15 << 20) + (512 << 10);
constexpr int WS_BYTES = 20 << 20;
constexpr unsigned SENT = 0xFFFFFFFFu;

DI f32x16 mfma(v4u a, v4u b, f32x16 c) {
  return __builtin_amdgcn_mfma_f32_32x32x16_bf16(__builtin_bit_cast(bf16x8, a), __builtin_bit_cast(bf16x8, b), c, 0, 0, 0);
}
DI f32x16 mfma32(float a, float b, f32x16 c) { return __builtin_amdgcn_mfma_f32_32x32x2f32(a, b, c, 0, 0, 0); }
DI unsigned pk2(float a, float b) { bf16x2 v; v[0] = (__bf16)a; v[1] = (__bf16)b; return __builtin_bit_cast(unsigned, v); }
DI float blo(unsigned v) { return __uint_as_float(v << 16); }
DI float bhi(unsigned v) { return __uint_as_float(v & 0xffff0000u); }
DI v4u pack8(const f32x16& x, int s) {
  return v4u{pk2(x[8 * s + 0], x[8 * s + 1]), pk2(x[8 * s + 2], x[8 * s + 3]), pk2(x[8 * s + 4], x[8 * s + 5]), pk2(x[8 * s + 6], x[8 * s + 7])};
}
DI v4u nosent(v4u v) {
#pragma unroll
  for (int i = 0; i < 4; ++i) v[i] = (v[i] == SENT) ? 0xFFFEFFFEu : v[i];
  return v;
}
DI bool hassent(v4u v) { return (v[0] == SENT) | (v[1] == SENT) | (v[2] == SENT) | (v[3] == SENT); }
DI float xsum(float v) { auto r = __builtin_amdgcn_permlane32_swap(__float_as_uint(v), __float_as_uint(v), false, false); return __uint_as_float(r[0]) + __uint_as_float(r[1]); }
DI float vmax(float a, float b) { return __builtin_elementwise_maximum(a, b); }
DI v2u trread(LAS char* p) { return __builtin_bit_cast(v2u, __builtin_amdgcn_ds_read_tr16_b64_v4i16((LAS s16x4*)p)); }
DI v4u tr2(LAS char* a, int d) { v2u l = trread(a), h = trread(a + d); return v4u{l[0], l[1], h[0], h[1]}; }
DI f32x16 zero16() { f32x16 z; for (int i = 0; i < 16; ++i) z[i] = 0.f; return z; }
DI float diag16(const f32x16& d, int lane) {
  const int r = lane & 31, hh = lane >> 5;
  const int istar = (r & 3) + 4 * (r >> 3);
  float v = 0.f;
#pragma unroll
  for (int i = 0; i < 16; ++i) v = (i == istar) ? d[i] : v;
  v = (hh == ((r >> 2) & 1)) ? v : 0.f;
  return xsum(v);
}
DI void dual_softmax_sc(f32x16& p, float sc) {
#pragma unroll
  for (int hf = 0; hf < 2; ++hf) {
    float m = vmax(vmax(vmax(p[8 * hf], p[8 * hf + 1]), vmax(p[8 * hf + 2], p[8 * hf + 3])), vmax(vmax(p[8 * hf + 4], p[8 * hf + 5]), vmax(p[8 * hf + 6], p[8 * hf + 7])));
    { auto rr = __builtin_amdgcn_permlane32_swap(__float_as_uint(m), __float_as_uint(m), false, false); m = vmax(__uint_as_float(rr[0]), __uint_as_float(rr[1])); }
    m = -(m * sc);
    float s = 0.f;
#pragma unroll
    for (int i = 0; i < 8; ++i) { float e = __builtin_amdgcn_exp2f(__builtin_fmaf(p[8 * hf + i], sc, m)); p[8 * hf + i] = e; s += e; }
    s = xsum(s);
    const float inv = __builtin_amdgcn_rcpf(s);
#pragma unroll
    for (int i = 0; i < 8; ++i) p[8 * hf + i] *= inv;
  }
}
DI void glds3(unsigned voff, const void* sbase, unsigned lds_dst) {
  unsigned keep;
  asm volatile("s_mov_b32 %0, m0\n\ts_mov_b32 m0, %3\n\ts_nop 0\n\tglobal_load_lds_dwordx4 %1, %2\n\tglobal_load_lds_dwordx4 %1, %2 offset:1024\n\t"
               "global_load_lds_dwordx4 %1, %2 offset:2048\n\ts_mov_b32 m0, %0" : "=&s"(keep) : "v"(voff), "s"(sbase), "s"(lds_dst) : "memory");
}
DI void glds1(unsigned voff, const void* sbase, unsigned lds_dst) {
  unsigned keep;
  asm volatile("s_mov_b32 %0, m0\n\ts_mov_b32 m0, %3\n\ts_nop 0\n\tglobal_load_lds_dwordx4 %1, %2\n\ts_mov_b32 m0, %0" : "=&s"(keep) : "v"(voff), "s"(sbase), "s"(lds_dst) : "memory");
}
DI void glds1c(unsigned voff, const void* sbase, unsigned lds_dst) {
  unsigned keep;
  asm volatile("s_mov_b32 %0, m0\n\ts_mov_b32 m0, %3\n\ts_nop 0\n\tglobal_load_lds_dwordx4 %1, %2 sc1\n\ts_mov_b32 m0, %0" : "=&s"(keep) : "v"(voff), "s"(sbase), "s"(lds_dst) : "memory");
}
#define SB() __builtin_amdgcn_sched_barrier(0)
#define LGKM0() asm volatile("s_waitcnt lgkmcnt(0)" ::: "memory")
#define BAR() __builtin_amdgcn_s_barrier()
#define ONES4 v4u{0x3f803f80u, 0x3f803f80u, 0x3f803f80u, 0x3f803f80u}
DI int pos_c(int T, int j) {
  const int e = T - 32;
  const int jo = T < 32 ? ((T >> 2) & 3) : (e & 3), idx = T < 32 ? ((T & 3) + ((T >> 4) << 2)) : (8 + (e >> 2));
  const int d = (jo - j) & 3;
  return 11 * d - ((j + d > 3) ? 1 : 0) + idx;
}

constexpr int K1_WL = 0;
constexpr int K1_PK = 0;
constexpr int K1_V = 16384;
constexpr int K1_WV = 98304;
constexpr int K1_CS = 147456;
constexpr int K1_MS = 150528;
constexpr int K1_C1 = 154624;
constexpr int K1_LDS = 156672;

__global__ __launch_bounds__(512, 2) void k1(const unsigned short* __restrict__ x, const unsigned short* __restrict__ wqkv,
                                             const unsigned short* __restrict__ wphi, const unsigned short* __restrict__ wo,
                                             const unsigned short* __restrict__ w1, const unsigned short* __restrict__ w2, char* __restrict__ ws) {
  extern __shared__ __attribute__((aligned(16))) char sm_[];
  LAS char* sm = (LAS char*)sm_;
  const unsigned smb = (unsigned)(size_t)sm_;
  const int lane = threadIdx.x & 63, wv = __builtin_amdgcn_readfirstlane(threadIdx.x >> 6);
  const int tg = wv & 3, KH = wv >> 2;
  const int r = lane & 31, hh = lane >> 5;
  const int b = blockIdx.x, xcd = b & 7, bi = b >> 3;
  const int tt = 4 * (xcd >> 1) + (bi & 3), g = 8 * (xcd & 1) + (bi >> 2);
  const int row8 = lane >> 3, part = lane & 7, p4 = lane & 3;
  const __amdgpu_buffer_rsrc_t rs = __builtin_amdgcn_make_buffer_rsrc((void*)ws, 0, WS_BYTES, 0x00020000);
  const int l16 = lane * 16;
  STAMP_DECL; STAMP(0);
  v4u xr[16];
  const unsigned xvo = row8 * 1024 + part * 16;
  const char* xsb = (const char*)x + (size_t)(tt * 128 + tg * 32) * 1024 + KH * 512;
  const unsigned wvo = (lane >> 2) * 3072 + p4 * 16;
  const char* wsb = (const char*)wqkv + (size_t)(256 * KH + 16 * tg) * 3072 + g * 64;
  const unsigned wld = smb + K1_WL + ((16 * KH + tg) * 3) * 1024;
#define XLOAD1(R, rg) asm volatile("global_load_dwordx4 %0, %1, %2 offset:%3" : "=v"(xr[4 * (R) + (rg)]) : "v"(xvo), "s"(xsb + (rg) * 8192), "n"((R) * 128) : "memory")
#define XLOAD(R) do { XLOAD1(R, 0); XLOAD1(R, 1); XLOAD1(R, 2); XLOAD1(R, 3); } while (0)
#define WLOAD(R) glds3(wvo, wsb + (size_t)(R) * 64 * 3072, wld + (R) * 12288)
  XLOAD(0); WLOAD(0); XLOAD(1);
  STAMP(1);
  const bool isw1 = b < 86;
  int rt_dst[2];
  f32x16 aq = zero16(), ak = zero16(), av = zero16(), c0 = zero16(), c1 = zero16(), c2 = zero16();
  const v4u ones = ONES4;
  float sa = 0.f, sb = 0.f, qa = 0.f, qb = 0.f;
  const int bl = (lane >> 4) & 1, q4 = (lane & 15) >> 2;
  LAS char* ap0 = sm + K1_WL + (16 * KH) * 3072 + (8 * hh + q4) * 64 + (bl ^ KH) * 32 + p4 * 8;
  LAS char* ap[4] = {ap0 + tg * 3072, ap0 + (tg ^ 1) * 3072, ap0 + (tg ^ 2) * 3072, ap0 + (tg ^ 3) * 3072};
  LAS char* stg = sm + K1_WV + wv * 6144;
  LAS char* stw0 = stg + row8 * 128 + ((part ^ (row8 >> 1)) << 4);
  LAS char* stw1 = stg + row8 * 128 + ((part ^ (row8 >> 1) ^ 4) << 4);
  const unsigned srd = (r * 128 + ((hh ^ ((r >> 1) & 7)) << 4)) ^ (tg << 5);
#define XWAIT(R, N) asm volatile("s_waitcnt vmcnt(%4)" : "+v"(xr[4 * (R)]), "+v"(xr[4 * (R) + 1]), "+v"(xr[4 * (R) + 2]), "+v"(xr[4 * (R) + 3]) : "n"(N) : "memory")
#define STW(R, k) *(LAS v4u*)(((k) & 1 ? stw1 : stw0) + (k) * 1024) = xr[4 * (R) + (k)]
#define STR(XF, k) XF[k] = *(LAS v4u*)(stg + (srd ^ ((k) << 5)))
#define STAT(v) do { const float lo_ = blo(v), hi_ = bhi(v); sa += lo_; sb += hi_; qa = __builtin_fmaf(lo_, lo_, qa); qb = __builtin_fmaf(hi_, hi_, qb); \
    asm volatile("" : "+v"(sa), "+v"(sb), "+v"(qa), "+v"(qb)); } while (0)
  v4u xf[2][4];
  XWAIT(0, 7);
  STW(0, 0); STW(0, 1); STW(0, 2); STW(0, 3); STR(xf[0], 0); STR(xf[0], 1); STR(xf[0], 2); STR(xf[0], 3);
  asm volatile("s_waitcnt vmcnt(4)" ::: "memory");
#pragma unroll
  for (int rho = 0; rho < 4; ++rho) {
    BAR();
    STAMP(2 + rho);
    v4u f[2][3];
#pragma unroll
    for (int c = 0; c < 3; ++c) f[0][c] = tr2(ap[0] + rho * 12288 + c * 1024, 256);
    SB();
#pragma unroll
    for (int u = 0; u < 4; ++u) {
      const v4u xb = xf[rho & 1][u];
#pragma unroll
      for (int c = 0; c < 3; ++c) {
        if (c == 0) aq = mfma(f[u & 1][0], xb, aq);
        if (c == 1) ak = mfma(f[u & 1][1], xb, ak);
        if (c == 2) av = mfma(f[u & 1][2], xb, av);
        if (u < 3) f[(u + 1) & 1][c] = tr2(ap[u + 1] + rho * 12288 + c * 1024, 256);
        STAT(xb[c]);
        if (rho < 3 && u == 0 && c == 0) WLOAD(rho + 1);
        if (rho < 2 && u == 0 && c == 1) { XLOAD1(rho + 2, 0); XLOAD1(rho + 2, 1); }
        if (rho < 2 && u == 0 && c == 2) { XLOAD1(rho + 2, 2); XLOAD1(rho + 2, 3); }
        if (rho == 2 && u == 0 && c == 1) {
#pragma unroll
          for (int u2 = 0; u2 < 2; ++u2) {
            const int i = 2 * wv + u2;
            const char* src; int dst;
            if (isw1) {
              const int T = b >> 1, s = 16 * (b & 1) + i, e = T - 32;
              const int q = T < 32 ? (T & 15) : (4 * (e & 3) + (e >> 2)), t = T < 32 ? (T >> 4) : 2;
              const int qp = s >> 1, p = ((((qp >> 2) - (q >> 2)) & 3) << 2) + (qp & 3);
              src = (const char*)w1 + (size_t)s * 44032 + T * 64;
              dst = W1T_OFF + (((q * 2 + (s & 1)) * 16 + p) * 3 + t) * 1024;
            } else {
              int id = (b - 86) * 12 + i;
              const bool ok = (i < 12) && (id < 1888);
              id = ok ? id : 0;
              const int rot = id & 1, q = (id >> 1) & 15, k = id >> 5;
              if (k < 16) { src = (const char*)wo + (16 * rot + k) * 16384 + q * 64; dst = WOT_OFF + ((q * 2 + rot) * 16 + k) * 1024; }
              else { const int T = k - 16; src = (const char*)w2 + (2 * T + rot) * 16384 + q * 64; dst = W2T_OFF + ((q * 2 + rot) * 43 + pos_c(T, q >> 2)) * 1024; }
              if (!ok) dst = DUMMY_OFF;
            }
            rt_dst[u2] = dst;
            glds1((lane >> 2) * (isw1 ? 2752 : 1024) + p4 * 16, src, smb + K1_WV + wv * 6144 + 4096 + u2 * 1024);
          }
        }
        if (rho < 3 && u == 2 && c == 0) { XWAIT(rho + 1, rho == 2 ? 5 : 7); STW(rho + 1, 0); }
        if (rho < 3 && u == 2 && c == 1) { STW(rho + 1, 1); }
        if (rho < 3 && u == 2 && c == 2) { STW(rho + 1, 2); }
        if (rho < 3 && u == 3 && c == 0) { STW(rho + 1, 3); }
        if (rho < 3 && u == 3 && c == 1) { STR(xf[(rho + 1) & 1], 0); STR(xf[(rho + 1) & 1], 1); }
        if (rho < 3 && u == 3 && c == 2) { STR(xf[(rho + 1) & 1], 2); STR(xf[(rho + 1) & 1], 3); }
        SB();
        if (u == 0) {
          if (c == 0) c0 = mfma(ones, f[0][0], c0);
          if (c == 1) c1 = mfma(ones, f[0][1], c1);
          if (c == 2) c2 = mfma(ones, f[0][2], c2);
          if (c == 2) STAT(xb[3]);
          SB();
        }
      }
      if (u > 0) { STAT(xb[3]); SB(); }
    }
    if (rho < 3) asm volatile("s_waitcnt vmcnt(%0)" :: "n"(rho == 2 ? 2 : 4) : "memory");
  }
  STAMP(6);
  unsigned we[8];
  {
    asm volatile("s_waitcnt vmcnt(0)" ::: "memory");
    LAS char* ra = stg + 4096 + (4 * hh + q4) * 64 + p4 * 8;
    const v4u f0 = tr2(ra + bl * 32, 512), f1 = tr2(ra + 1024 + (bl ^ 1) * 32, 512);
    __builtin_amdgcn_raw_buffer_store_b128(f0, rs, l16, rt_dst[0], 0);
    __builtin_amdgcn_raw_buffer_store_b128(f1, rs, l16, rt_dst[1], 0);
#pragma unroll
    for (int j = 0; j < 8; ++j) we[j] = wphi[((2 * g + KH) * 16 + 8 * (j >> 2) + 4 * hh + (j & 3)) * 16 + (r & 15)];
    if (isw1) {
      const f32x16 ca = mfma(ones, f0, zero16()), cb = mfma(ones, f1, zero16());
      if (hh == 0) { ((LAS float*)(sm + K1_C1))[wv * 64 + r] = ca[0]; ((LAS float*)(sm + K1_C1))[wv * 64 + 32 + (r ^ 16)] = cb[0]; }
    }
  }
  *(LAS f32x2*)(sm + K1_MS + (tg * 32 + r) * 32 + KH * 16 + hh * 8) = f32x2{sa + sb, qa + qb};
  if (hh == 0) { LAS float* cs = (LAS float*)(sm + K1_CS) + (r ^ (16 * KH)) * 8 + wv; cs[0] = c0[0]; cs[256] = c1[0]; cs[512] = c2[0]; }
  {
    LAS f32x4* ex = (LAS f32x4*)(stg) + lane;
    ex[0] = f32x4{aq[8], aq[9], aq[10], aq[11]}; ex[64] = f32x4{aq[12], aq[13], aq[14], aq[15]};
    ex[128] = f32x4{ak[8], ak[9], ak[10], ak[11]}; ex[192] = f32x4{ak[12], ak[13], ak[14], ak[15]};
    ex[256] = f32x4{av[8], av[9], av[10], av[11]}; ex[320] = f32x4{av[12], av[13], av[14], av[15]};
  }
  LGKM0();
  BAR();
  STAMP(7);
  float rstd;
  {
    LAS f32x4* ex = (LAS f32x4*)(sm + K1_WV + (wv ^ 4) * 6144) + lane;
    LAS f32x4* sp = (LAS f32x4*)(sm + K1_MS + (tg * 32 + r) * 32);
    LAS f32x4* cp = (LAS f32x4*)(sm + K1_CS + (r ^ (16 * KH)) * 32);
    const f32x4 st = sp[0] + sp[1];
    const f32x4 cb = cp[64] + cp[65];
    const f32x4 e2 = ex[128], e3 = ex[192];
    const float mean = (st[0] + st[2]) * (1.0f / 512.0f);
    const float hz = hh ? 0.f : 1.f;
    const float cs1 = ((cb[0] + cb[1]) + (cb[2] + cb[3])) * hz;
#pragma unroll
    for (int e = 0; e < 4; ++e) { ak[e] += e2[e]; ak[4 + e] += e3[e]; }
    ak = mfma32(cs1, -mean, ak);
    const f32x4 ca = cp[0] + cp[1], cc = cp[128] + cp[129];
    const f32x4 e0 = ex[0], e1 = ex[64], e4 = ex[256], e5 = ex[320];
    const float cs0 = ((ca[0] + ca[1]) + (ca[2] + ca[3])) * hz, cs2 = ((cc[0] + cc[1]) + (cc[2] + cc[3])) * hz;
#pragma unroll
    for (int e = 0; e < 4; ++e) { aq[e] += e0[e]; aq[4 + e] += e1[e]; av[e] += e4[e]; av[4 + e] += e5[e]; }
    const float var = vmax((st[1] + st[3]) * (1.0f / 512.0f) - mean * mean, 0.f);
    rstd = __builtin_amdgcn_rsqf(var + 1e-5f);
    av = mfma32(cs2, -mean, av);
    aq = mfma32(cs0, -mean, aq);
  }
  const unsigned sg = (r >= 16) ? 0x80008000u : 0u;
  const v4u wp = v4u{(we[0] | (we[1] << 16)) ^ sg, (we[2] | (we[3] << 16)) ^ sg, (we[4] | (we[5] << 16)) ^ sg, (we[6] | (we[7] << 16)) ^ sg};
  f32x16 pk = mfma(wp, pack8(ak, 0), zero16());
  {
    LAS char* vi = sm + K1_V + tg * 2048 + r * 64 + KH * 32 + hh * 8;
    *(LAS v2u*)(vi) = v2u{pk2(av[0] * rstd, av[1] * rstd), pk2(av[2] * rstd, av[3] * rstd)};
    *(LAS v2u*)(vi + 16) = v2u{pk2(av[4] * rstd, av[5] * rstd), pk2(av[6] * rstd, av[7] * rstd)};
  }
  dual_softmax_sc(pk, rstd * 1.4426950408889634f);
  {
    LAS char* pi = sm + K1_PK + (tg * 2 + KH) * 2048 + r * 64 + hh * 8;
#pragma unroll
    for (int gq = 0; gq < 4; ++gq) *(LAS v2u*)(pi + gq * 16) = v2u{pk2(pk[4 * gq], pk[4 * gq + 1]), pk2(pk[4 * gq + 2], pk[4 * gq + 3])};
  }
  LGKM0();
  BAR();
  STAMP(8);
  if (tg == 0) {
    f32x16 z = zero16();
    const bool isv = ((r >> 4) == KH);
    LAS char* pa = sm + K1_PK + KH * 2048 + (8 * hh + q4) * 64 + bl * 32 + p4 * 8;
    LAS char* pv = sm + K1_V + (8 * hh + q4) * 64 + bl * 32 + p4 * 8;
#pragma unroll
    for (int ks = 0; ks < 8; ++ks) {
      const v4u a = tr2(pa + (ks >> 1) * 4096 + (ks & 1) * 1024, 256);
      v4u bv = tr2(pv + ks * 1024, 256);
      if (!isv) bv = ones;
      z = mfma(a, bv, z);
    }
    if (isv || ((r & 15) == 0)) {
      GAS f32x4* dst = (GAS f32x4*)(ws + KVP_OFF + (size_t)(tt * 32 + 2 * g + KH) * 2176 + (isv ? (r & 15) : 16) * 128 + hh * 64);
#pragma unroll
      for (int gq = 0; gq < 4; ++gq) dst[gq] = f32x4{z[4 * gq], z[4 * gq + 1], z[4 * gq + 2], z[4 * gq + 3]};
    }
  } else if (tg == 1) {
    if (KH == 0 && isw1 && lane < 32) {
      LAS float* cp = (LAS float*)(sm + K1_C1) + lane;
      float s = 0.f;
#pragma unroll
      for (int k = 0; k < 16; ++k) s += cp[32 * k];
      ((GAS float*)(ws + CS1_OFF))[b * 32 + lane] = s;
    }
    if (KH == 1) { __builtin_amdgcn_raw_buffer_store_b128(v4u{SENT, SENT, SENT, SENT}, rs, l16, KVF_OFF + b * 1024, 0); if (b < 32) __builtin_amdgcn_raw_buffer_store_b128(v4u{SENT, SENT, SENT, SENT}, rs, l16, KVF_OFF + (256 + b) * 1024, 0); }
    if (KH == 1 && lane < 4) ((gu32*)(ws + FLG_OFF))[(b * 4 + lane) * 16] = 0u;
  }
  {
    f32x16 pq = mfma(wp, pack8(aq, 0), zero16());
    dual_softmax_sc(pq, rstd * 1.4426950408889634f);
    const int qo = QF_OFF + ((tt * 4 + tg) * 32 + 2 * g + KH) * 2048;
    __builtin_amdgcn_raw_buffer_store_b128(pack8(pq, 0), rs, l16, qo, 0);
    __builtin_amdgcn_raw_buffer_store_b128(pack8(pq, 1), rs, l16, qo + 1024, 0);
  }
  STAMP(9); STAMP_FLUSH(0);
}

constexpr int K2_OF = 0;
constexpr int K2_HF = 32768;
constexpr int K2_H1 = 65536;
constexpr int K2_ST = 153600;
constexpr int K2_LDS = 155648;
#define KV_GATE 220
constexpr int RD = 28;
constexpr int NA = 16, NB = 48, NC = 43, NTOT = NA + NB + NC;

DI void wait_flags(gu32* f, int j, int lane) {
  const bool need = lane < 4 && lane != j;
#pragma unroll 1
  for (unsigned sp = 0; sp < (1u << 22); ++sp) {
    unsigned v = 1u;
    if (need) v = __hip_atomic_load(f + lane * 16, __ATOMIC_RELAXED, __HIP_MEMORY_SCOPE_AGENT);
    if (__builtin_amdgcn_ballot_w64(v == 0u) == 0ull) break;
    __builtin_amdgcn_s_sleep(1);
  }
  __builtin_amdgcn_fence(__ATOMIC_ACQUIRE, "wavefront");
}

__global__ __launch_bounds__(512, 2) void k2(const unsigned short* __restrict__ x, const unsigned short* __restrict__ b1,
                                             const unsigned short* __restrict__ b2, char* __restrict__ ws, unsigned short* __restrict__ out) {
  extern __shared__ __attribute__((aligned(16))) char sm_[];
  LAS char* sm = (LAS char*)sm_;
  const int lane = threadIdx.x & 63, wv = __builtin_amdgcn_readfirstlane(threadIdx.x >> 6);
  const int w = wv & 3, KH = wv >> 2;
  const int r = lane & 31, hh = lane >> 5;
  const int b = blockIdx.x, c = (b & 7) * 8 + (b >> 5), j = (b >> 3) & 3, q = 4 * j + w;
  const bool three = (4 * w + j) < 11;
  const __amdgpu_buffer_rsrc_t rs = __builtin_amdgcn_make_buffer_rsrc((void*)ws, 0, WS_BYTES, 0x00020000);
  const int l16 = lane * 16;
  STAMP_DECL; STAMP(0);
  const unsigned long long tstart = __builtin_amdgcn_s_memrealtime();
  const bool red = (b < 136) && (KH == 0);
  f32x4 kp[2] = {{0.f, 0.f, 0.f, 0.f}, {0.f, 0.f, 0.f, 0.f}};
  if (red) {
    const GAS f32x4* pp = (const GAS f32x4*)(ws + KVP_OFF + (size_t)(lane >> 2) * 69632) + (b * 4 + w) * 8 + (lane & 3);
    kp[0] = pp[0]; kp[1] = pp[4];
  }
  const int rr = r ^ (16 * KH);
  const int T2 = three ? (32 + 4 * w + j) : q;
  float csa[3], csb[3]; unsigned bb1[3];
#pragma unroll
  for (int t = 0; t < 3; ++t) {
    const int T = t < 2 ? q + 16 * t : T2;
    const GAS float* cp = (const GAS float*)(ws + CS1_OFF) + T * 64 + rr;
    csa[t] = cp[0]; csb[t] = cp[32]; bb1[t] = b1[32 * T + rr];
  }
  const unsigned bb2 = b2[128 * j + 32 * w + r];
  v4u pf[4][2];
#pragma unroll
  for (int hq = 0; hq < 4; ++hq)
#pragma unroll
    for (int s = 0; s < 2; ++s) pf[hq][s] = __builtin_amdgcn_raw_buffer_load_b128(rs, l16, QF_OFF + ((c * 32 + 4 * wv + hq) * 2 + s) * 1024, 0);
  v4u ring[RD];
  const int sA = WOT_OFF + (q * 2 + KH) * 16384, sB = W1T_OFF + (q * 2 + KH) * 49152, sC = W2T_OFF + (q * 2 + KH) * 44032;
  const bool comm = (w == 3);
  constexpr int OOB = 0x7FFFFF00;
  const int l3 = three ? l16 : OOB;
  const int lq = comm ? OOB : l16;
  const int lq3 = comm ? OOB : l3;
#define ISB3(i) ((i) >= NA && (i) < NA + NB && ((i) - NA) % 3 == 2)
#define ROFF(i) ((i) < NA ? sA + (i) * 1024 : (i) < NA + NB ? sB + ((i) - NA) * 1024 : sC + ((i) - NA - NB) * 1024)
#define RLOAD(i) do { if ((i) < NTOT) ring[(i) % RD] = __builtin_amdgcn_raw_buffer_load_b128(rs, ISB3(i) ? l3 : l16, ROFF(i), 0); } while (0)
#define RLOADQ(i) do { if ((i) < NTOT) ring[(i) % RD] = __builtin_amdgcn_raw_buffer_load_b128(rs, ISB3(i) ? lq3 : lq, ROFF(i), 0); } while (0)
#define RLOADS(lo, hi) _Pragma("unroll") for (int i_ = (lo); i_ < (hi); ++i_) RLOAD(i_)
  const int n0 = 128 * j + 32 * w + 16 * KH + 4 * hh;
  const v2u xa = *(const GAS v2u*)((const char*)x + (size_t)(32 * c + r) * 1024 + n0 * 2);
  const v2u xb2 = *(const GAS v2u*)((const char*)x + (size_t)(32 * c + r) * 1024 + n0 * 2 + 16);
  RLOADS(0, 6);
  SB();
  if (red) {
#pragma unroll
    for (int k = 0; k < 2; ++k) {
#pragma unroll
      for (int e = 0; e < 4; ++e) {
        float v = kp[k][e];
        v += __builtin_bit_cast(float, __builtin_amdgcn_update_dpp(0, __builtin_bit_cast(int, v), 0x124, 0xf, 0xf, false));
        v += __builtin_bit_cast(float, __builtin_amdgcn_update_dpp(0, __builtin_bit_cast(int, v), 0x128, 0xf, 0xf, false));
        { auto s2 = __builtin_amdgcn_permlane16_swap(__float_as_uint(v), __float_as_uint(v), false, false); v = __uint_as_float(s2[0]) + __uint_as_float(s2[1]); }
        kp[k][e] = xsum(v);
      }
    }
    const f32x4 kk = (lane & 4) ? kp[1] : kp[0];
    unsigned v0 = pk2(kk[0], kk[1]), v1 = pk2(kk[2], kk[3]);
    v0 = (v0 == SENT) ? 0xFFFEFFFEu : v0; v1 = (v1 == SENT) ? 0xFFFEFFFEu : v1;
    __hip_atomic_store((GAS unsigned long long*)(ws + KVF_OFF + (lane >> 3) * KVF_REP) + (b * 4 + w) * 8 + (lane & 7), (unsigned long long)v0 | ((unsigned long long)v1 << 32), __ATOMIC_RELAXED, __HIP_MEMORY_SCOPE_AGENT);
  }
  RLOADS(6, 8);
  SB();
  STAMP(1);
  while (__builtin_amdgcn_s_memrealtime() - tstart < KV_GATE) __builtin_amdgcn_s_sleep(1);
  v4u kv[4][2];
  {
    const int ko = (r < 16) ? r * 64 + hh * 32 : ((r == 16) | (r == 20)) ? 1024 + hh * 32 : OOB;
#pragma unroll 1
    for (unsigned sp = 0; sp < (1u << 22); ++sp) {
      bool bad = false;
#pragma unroll
      for (int hq = 0; hq < 4; ++hq)
#pragma unroll
        for (int s = 0; s < 2; ++s) {
          kv[hq][s] = __builtin_amdgcn_raw_buffer_load_b128(rs, ko + s * 16, KVF_OFF + (b & 7) * KVF_REP + (4 * wv + hq) * 1088, 16);
          bad |= hassent(kv[hq][s]);
        }
      if (__builtin_amdgcn_ballot_w64(bad) == 0ull) break;
      __builtin_amdgcn_s_sleep(4);
    }
  }
  float ab1[3], ab2 = (hh | KH) ? 0.f : blo(bb2);
#pragma unroll
  for (int t = 0; t < 3; ++t) ab1[t] = hh ? blo(bb1[t]) : csa[t] + csb[t];
  asm volatile("" : "+v"(ab1[0]), "+v"(ab1[1]), "+v"(ab1[2]), "+v"(ab2));
  SB();
  STAMP(2);
#pragma unroll
  for (int hq = 0; hq < 4; ++hq) {
    f32x16 d = mfma(kv[hq][0], pf[hq][0], zero16());
    d = mfma(kv[hq][1], pf[hq][1], d);
    RLOADS(8 + 5 * hq, 13 + 5 * hq);
    const float inv = __builtin_amdgcn_rcpf(d[8] + 1e-6f);
#pragma unroll
    for (int i = 0; i < 8; ++i) d[i] *= inv;
    *(LAS v4u*)(sm + K2_OF + (4 * wv + hq) * 1024 + l16) = pack8(d, 0);
  }
  LGKM0();
  BAR();
  STAMP(3);
  v4u hfrag;
  LAS char* hf = sm + K2_HF + KH * 16384 + l16;
  {
    f32x16 acc = zero16();
    acc[0] = blo(xa[0]); acc[1] = bhi(xa[0]); acc[2] = blo(xa[1]); acc[3] = bhi(xa[1]);
    acc[4] = blo(xb2[0]); acc[5] = bhi(xb2[0]); acc[6] = blo(xb2[1]); acc[7] = bhi(xb2[1]);
    LAS char* ob = sm + K2_OF + KH * 16384 + l16;
#pragma unroll
    for (int i = 0; i < NA; ++i) { acc = mfma(ring[i % RD], *(LAS v4u*)(ob + i * 1024), acc); RLOADQ(i + RD); SB(); }
    LAS f32x4* ex = (LAS f32x4*)(sm + K2_H1 + wv * 2048) + lane;
    ex[0] = f32x4{acc[8], acc[9], acc[10], acc[11]}; ex[64] = f32x4{acc[12], acc[13], acc[14], acc[15]};
    LGKM0();
    BAR();
    LAS f32x4* ey = (LAS f32x4*)(sm + K2_H1 + (wv ^ 4) * 2048) + lane;
    const f32x4 e0 = ey[0], e1 = ey[64];
#pragma unroll
    for (int e = 0; e < 4; ++e) { acc[e] += e0[e]; acc[4 + e] += e1[e]; }
    hfrag = pack8(acc, 0);
    *(LAS v4u*)(hf + w * 1024) = hfrag;
  }
  LGKM0();
  BAR();
  STAMP(4);
  gu32* fl0 = (gu32*)(ws + FLG_OFF) + ((c * 2 + KH) * 4) * 16;
  if (comm) {
#pragma unroll
    for (int k = 0; k < 4; ++k)
      __builtin_amdgcn_raw_buffer_store_b128(*(LAS v4u*)(hf + k * 1024), rs, l16, HX_OFF + (c * 32 + 2 * (4 * j + k) + KH) * 1024, 16);
    asm volatile("s_waitcnt vmcnt(0)" ::: "memory");
    if (lane == 0) __hip_atomic_store(fl0 + j * 16, 1u, __ATOMIC_RELAXED, __HIP_MEMORY_SCOPE_AGENT);
  }
  f32x16 ac[3];
#pragma unroll
  for (int t = 0; t < 3; ++t) ac[t] = zero16();
  constexpr int QB = 9;
#define BSTEP(p, Q) do { const v4u hb_ = *(LAS v4u*)(hf + (p) * 1024); _Pragma("unroll") for (int t_ = 0; t_ < 3; ++t_) { \
    ac[t_] = mfma(ring[(NA + (p) * 3 + t_) % RD], hb_, ac[t_]); if (Q) RLOADQ(NA + (p) * 3 + t_ + RD); else RLOAD(NA + (p) * 3 + t_ + RD); SB(); } } while (0)
  BSTEP(0, 1); BSTEP(1, 1); BSTEP(2, 1); BSTEP(3, 1);
  if (comm) {
    wait_flags(fl0, j, lane);
    const unsigned hfl = (unsigned)(size_t)sm_ + K2_HF + KH * 16384;
#pragma unroll
    for (int d = 1; d < 4; ++d)
#pragma unroll
      for (int k = 0; k < 4; ++k) glds1c(l16, ws + HX_OFF + (c * 32 + 2 * (4 * ((j + d) & 3) + k) + KH) * 1024, hfl + (4 * d + k) * 1024);
    RLOADS(RD, NA + 12 + RD);
    asm volatile("s_waitcnt vmcnt(28)" ::: "memory");
  }
  BAR();
  STAMP(5);
  {
    const v4u ones = ONES4;
    f32x16 ag = zero16(), as = zero16();
#pragma unroll
    for (int u = 0; u < 4; ++u) {
      const v4u hb = *(LAS v4u*)(sm + K2_HF + (4 * wv + u) * 1024 + l16);
      ag = mfma(hb, hb, ag);
      as = mfma(ones, hb, as);
    }
    const float dg = diag16(ag, lane);
    if (hh == 0) *(LAS f32x2*)(sm + K2_ST + r * 64 + wv * 8) = f32x2{as[0], dg};
  }
#pragma unroll
  for (int p = 4; p < 16; ++p) BSTEP(p, p >= 16 - QB / 3);
  STAMP(6);
  LAS char* h1 = sm + K2_H1 + KH * 44032 + l16;
  {
    LAS char* e01 = sm + K2_OF + wv * 4096 + l16;
    LAS char* e2 = sm + K2_H1 + KH * 44032 + 16384 + w * 2048 + l16;
#pragma unroll
    for (int t = 0; t < 3; ++t) {
      LAS char* d = t < 2 ? e01 + t * 2048 : e2;
      *(LAS f32x4*)(d) = f32x4{ac[t][8], ac[t][9], ac[t][10], ac[t][11]};
      *(LAS f32x4*)(d + 1024) = f32x4{ac[t][12], ac[t][13], ac[t][14], ac[t][15]};
    }
    LGKM0();
    BAR();
    LAS char* f01 = sm + K2_OF + (wv ^ 4) * 4096 + l16;
    LAS char* f2 = sm + K2_H1 + (KH ^ 1) * 44032 + 16384 + w * 2048 + l16;
    LAS f32x4* sp = (LAS f32x4*)(sm + K2_ST + r * 64);
    const f32x4 st = (sp[0] + sp[1]) + (sp[2] + sp[3]);
    const float mean = (st[0] + st[2]) * (1.0f / 512.0f);
    const float var = vmax((st[1] + st[3]) * (1.0f / 512.0f) - mean * mean, 0.f) + 1e-5f;
    const float rstd = __builtin_amdgcn_rsqf(var);
    const float bop = hh ? var * rstd : -mean;
    const float r2 = rstd * rstd;
#pragma unroll
    for (int t = 0; t < 3; ++t) {
      LAS char* d = t < 2 ? f01 + t * 2048 : f2;
      const f32x4 e0 = *(LAS f32x4*)(d), e1 = *(LAS f32x4*)(d + 1024);
#pragma unroll
      for (int e = 0; e < 4; ++e) { ac[t][e] += e0[e]; ac[t][4 + e] += e1[e]; }
      ac[t] = mfma32(ab1[t], bop, ac[t]);
    }
#pragma unroll
    for (int t = 0; t < 3; ++t) {
#pragma unroll
      for (int e = 0; e < 8; ++e) { const float p = vmax(ac[t][e], 0.f); ac[t][e] = (p * p) * r2; }
      if (t < 2 || three) *(LAS v4u*)(h1 + (4 * t + w) * 1024) = pack8(ac[t], 0);
    }
  }
  LGKM0();
  BAR();
  STAMP(7);
  gu32* fl1 = (gu32*)(ws + FLG_OFF + 32768) + ((c * 2 + KH) * 4) * 16;
  if (comm) {
#pragma unroll
    for (int k = 0; k < 11; ++k) {
      const int T = k < 4 ? 4 * j + k : k < 8 ? 16 + 4 * j + (k - 4) : 32 + 4 * (k - 8) + j;
      if (k < 10 || j < 3) __builtin_amdgcn_raw_buffer_store_b128(*(LAS v4u*)(h1 + k * 1024), rs, l16, H1X_OFF + (c * 86 + 2 * T + KH) * 1024, 16);
    }
    asm volatile("s_waitcnt vmcnt(0)" ::: "memory");
    if (lane == 0) __hip_atomic_store(fl1 + j * 16, 1u, __ATOMIC_RELAXED, __HIP_MEMORY_SCOPE_AGENT);
  }
  f32x16 acc = mfma32(ab2, 1.0f, zero16());
#pragma unroll
  for (int e = 0; e < 4; ++e) { acc[2 * e] += blo(hfrag[e]); acc[2 * e + 1] += bhi(hfrag[e]); }
#define CSTEP(p, Q) do { acc = mfma(ring[(NA + NB + (p)) % RD], *(LAS v4u*)(h1 + (p) * 1024), acc); if (Q) RLOADQ(NA + NB + (p) + RD); else RLOAD(NA + NB + (p) + RD); SB(); } while (0)
#pragma unroll
  for (int p = 0; p < 10; ++p) CSTEP(p, 1);
  if (comm) {
    wait_flags(fl1, j, lane);
    const unsigned h1l = (unsigned)(size_t)sm_ + K2_H1 + KH * 44032;
#pragma unroll
    for (int d = 1; d < 4; ++d) {
      const int jp = (j + d) & 3;
      const int pb = 11 * d - ((j + d > 3) ? 1 : 0);
#pragma unroll
      for (int k = 0; k < 11; ++k) {
        const int T = k < 4 ? 4 * jp + k : k < 8 ? 16 + 4 * jp + (k - 4) : 32 + 4 * (k - 8) + jp;
        if (k < 10 || jp < 3) glds1c(l16, ws + H1X_OFF + (c * 86 + 2 * T + KH) * 1024, h1l + (pb + k) * 1024);
      }
    }
    asm volatile("s_waitcnt vmcnt(0)" ::: "memory");
    RLOADS(NA + NB - QB + RD, NA + NB + 10 + RD);
  }
  BAR();
  STAMP(8);
#pragma unroll
  for (int p = 10; p < NC; ++p) CSTEP(p, 0);
  STAMP(9);
  {
    LAS f32x4* ex = (LAS f32x4*)(sm + K2_OF + wv * 2048) + lane;
    ex[0] = f32x4{acc[8], acc[9], acc[10], acc[11]}; ex[64] = f32x4{acc[12], acc[13], acc[14], acc[15]};
    LGKM0();
    BAR();
    LAS f32x4* ey = (LAS f32x4*)(sm + K2_OF + (wv ^ 4) * 2048) + lane;
    const f32x4 e0 = ey[0], e1 = ey[64];
#pragma unroll
    for (int e = 0; e < 4; ++e) { acc[e] += e0[e]; acc[4 + e] += e1[e]; }
    const v4u o = pack8(acc, 0);
    auto r0 = __builtin_amdgcn_permlane32_swap(o[0], o[2], false, false);
    auto r1 = __builtin_amdgcn_permlane32_swap(o[1], o[3], false, false);
    *(GAS v4u*)((char*)out + (size_t)(32 * c + r) * 1024 + (128 * j + 32 * w + 16 * KH + 8 * hh) * 2) = v4u{r0[0], r1[0], r0[1], r1[1]};
  }
  STAMP(10); STAMP(11); STAMP_FLUSH(1);
}
}

extern "C" void kernel_launch(void* const* d_in, const int* in_sizes, int n_in, void* d_out, int out_size, void* d_ws, size_t ws_size,
                              hipStream_t stream) {
  const unsigned short* x = (const unsigned short*)d_in[0];
  const unsigned short* wqkv = (const unsigned short*)d_in[1];
  const unsigned short* wphi = (const unsigned short*)d_in[2];
  const unsigned short* wo = (const unsigned short*)d_in[3];
  const unsigned short* w1 = (const unsigned short*)d_in[5];
  const unsigned short* b1 = (const unsigned short*)d_in[6];
  const unsigned short* w2 = (const unsigned short*)d_in[7];
  const unsigned short* b2 = (const unsigned short*)d_in[8];
  char* ws = (char*)d_ws;
  (void)hipFuncSetAttribute((const void*)k1, hipFuncAttributeMaxDynamicSharedMemorySize, K1_LDS);
  (void)hipFuncSetAttribute((const void*)k2, hipFuncAttributeMaxDynamicSharedMemorySize, K2_LDS);
  k1<<<256, 512, K1_LDS, stream>>>(x, wqkv, wphi, wo, w1, w2, ws);
  k2<<<256, 512, K2_LDS, stream>>>(x, b1, b2, ws, (unsigned short*)d_out);
}
```

```cpp
#include <hip/hip_runtime.h>
#include <hip/hip_bf16.h>
#include <stdint.h>

#define GAS __attribute__((address_space(1)))
#define LAS __attribute__((address_space(3)))
#define DI __device__ __forceinline__
typedef unsigned v4u __attribute__((ext_vector_type(4)));
typedef unsigned v2u __attribute__((ext_vector_type(2)));
typedef float f32x16 __attribute__((ext_vector_type(16)));
typedef float f32x4 __attribute__((ext_vector_type(4)));
typedef float f32x2 __attribute__((ext_vector_type(2)));
typedef __bf16 bf16x8 __attribute__((ext_vector_type(8)));
typedef __bf16 bf16x2 __attribute__((ext_vector_type(2)));
typedef short s16x4 __attribute__((ext_vector_type(4)));
typedef GAS unsigned gu32;

#define STAMP_DECL
#define STAMP(i)
#define STAMP_FLUSH(k)

namespace {
constexpr int WOT_OFF = 0;
constexpr int W1T_OFF = 512 << 10;
constexpr int W2T_OFF = 2048 << 10;
constexpr int QF_OFF = 16 << 20;
constexpr int KVP_OFF = 6 << 20;
constexpr int KVF_OFF = (7 << 20) + (128 << 10);
constexpr int KVF_REP = 36864;
constexpr int CS1_OFF = (7 << 20) + (640 << 10);
constexpr int WPF_OFF = (7 << 20) + (704 << 10);
constexpr int DUMMY_OFF = (7 << 20) + (768 << 10);
constexpr int HX_OFF = 8 << 20;
constexpr int H1X_OFF = 10 << 20;
constexpr int FLG_OFF = (15 << 20) + (512 << 10);
constexpr int WS_BYTES = 20 << 20;
constexpr unsigned SENT = 0xFFFFFFFFu;

DI f32x16 mfma(v4u a, v4u b, f32x16 c) {
  return __builtin_amdgcn_mfma_f32_32x32x16_bf16(__builtin_bit_cast(bf16x8, a), __builtin_bit_cast(bf16x8, b), c, 0, 0, 0);
}
DI f32x16 mfma32(float a, float b, f32x16 c) { return __builtin_amdgcn_mfma_f32_32x32x2f32(a, b, c, 0, 0, 0); }
DI unsigned pk2(float a, float b) { bf16x2 v; v[0] = (__bf16)a; v[1] = (__bf16)b; return __builtin_bit_cast(unsigned, v); }
DI float blo(unsigned v) { return __uint_as_float(v << 16); }
DI float bhi(unsigned v) { return __uint_as_float(v & 0xffff0000u); }
DI v4u pack8(const f32x16& x, int s) {
  return v4u{pk2(x[8 * s + 0], x[8 * s + 1]), pk2(x[8 * s + 2], x[8 * s + 3]), pk2(x[8 * s + 4], x[8 * s + 5]), pk2(x[8 * s + 6], x[8 * s + 7])};
}
DI v4u nosent(v4u v) {
#pragma unroll
  for (int i = 0; i < 4; ++i) v[i] = (v[i] == SENT) ? 0xFFFEFFFEu : v[i];
  return v;
}
DI bool hassent(v4u v) { return (v[0] == SENT) | (v[1] == SENT) | (v[2] == SENT) | (v[3] == SENT); }
DI float xsum(float v) { auto r = __builtin_amdgcn_permlane32_swap(__float_as_uint(v), __float_as_uint(v), false, false); return __uint_as_float(r[0]) + __uint_as_float(r[1]); }
DI float vmax(float a, float b) { return __builtin_elementwise_maximum(a, b); }
DI v2u trread(LAS char* p) { return __builtin_bit_cast(v2u, __builtin_amdgcn_ds_read_tr16_b64_v4i16((LAS s16x4*)p)); }
DI v4u tr2(LAS char* a, int d) { v2u l = trread(a), h = trread(a + d); return v4u{l[0], l[1], h[0], h[1]}; }
DI f32x16 zero16() { f32x16 z; for (int i = 0; i < 16; ++i) z[i] = 0.f; return z; }
DI float diag16(const f32x16& d, int lane) {
  const int r = lane & 31, hh = lane >> 5;
  const int istar = (r & 3) + 4 * (r >> 3);
  float v = 0.f;
#pragma unroll
  for (int i = 0; i < 16; ++i) v = (i == istar) ? d[i] : v;
  v = (hh == ((r >> 2) & 1)) ? v : 0.f;
  return xsum(v);
}
DI void dual_softmax_sc(f32x16& p, float sc) {
#pragma unroll
  for (int hf = 0; hf < 2; ++hf) {
    float m = vmax(vmax(vmax(p[8 * hf], p[8 * hf + 1]), vmax(p[8 * hf + 2], p[8 * hf + 3])), vmax(vmax(p[8 * hf + 4], p[8 * hf + 5]), vmax(p[8 * hf + 6], p[8 * hf + 7])));
    { auto rr = __builtin_amdgcn_permlane32_swap(__float_as_uint(m), __float_as_uint(m), false, false); m = vmax(__uint_as_float(rr[0]), __uint_as_float(rr[1])); }
    m = -(m * sc);
    float s = 0.f;
#pragma unroll
    for (int i = 0; i < 8; ++i) { float e = __builtin_amdgcn_exp2f(__builtin_fmaf(p[8 * hf + i], sc, m)); p[8 * hf + i] = e; s += e; }
    s = xsum(s);
    const float inv = __builtin_amdgcn_rcpf(s);
#pragma unroll
    for (int i = 0; i < 8; ++i) p[8 * hf + i] *= inv;
  }
}
DI void glds3(unsigned voff, const void* sbase, unsigned lds_dst) {
  unsigned keep;
  asm volatile("s_mov_b32 %0, m0\n\ts_mov_b32 m0, %3\n\ts_nop 0\n\tglobal_load_lds_dwordx4 %1, %2\n\tglobal_load_lds_dwordx4 %1, %2 offset:1024\n\t"
               "global_load_lds_dwordx4 %1, %2 offset:2048\n\ts_mov_b32 m0, %0" : "=&s"(keep) : "v"(voff), "s"(sbase), "s"(lds_dst) : "memory");
}
DI void glds1(unsigned voff, const void* sbase, unsigned lds_dst) {
  unsigned keep;
  asm volatile("s_mov_b32 %0, m0\n\ts_mov_b32 m0, %3\n\ts_nop 0\n\tglobal_load_lds_dwordx4 %1, %2\n\ts_mov_b32 m0, %0" : "=&s"(keep) : "v"(voff), "s"(sbase), "s"(lds_dst) : "memory");
}
DI void glds1c(unsigned voff, const void* sbase, unsigned lds_dst) {
  unsigned keep;
  asm volatile("s_mov_b32 %0, m0\n\ts_mov_b32 m0, %3\n\ts_nop 0\n\tglobal_load_lds_dwordx4 %1, %2 sc1\n\ts_mov_b32 m0, %0" : "=&s"(keep) : "v"(voff), "s"(sbase), "s"(lds_dst) : "memory");
}
#define SB() __builtin_amdgcn_sched_barrier(0)
#define LGKM0() asm volatile("s_waitcnt lgkmcnt(0)" ::: "memory")
#define BAR() __builtin_amdgcn_s_barrier()
#define ONES4 v4u{0x3f803f80u, 0x3f803f80u, 0x3f803f80u, 0x3f803f80u}
DI int pos_c(int s, int j) {
  const int T = s >> 1;
  if (T >= 32) return s;
  const int st = T >> 4, d = (((T >> 2) & 3) - j) & 3, lo = 2 * (T & 3) + (s & 1);
  return d == 0 ? 8 * st + lo : 8 + 24 * st + 8 * d + lo;
}

constexpr int K1_WL = 0;
constexpr int K1_PK = 0;
constexpr int K1_V = 16384;
constexpr int K1_WV = 98304;
constexpr int K1_CS = 147456;
constexpr int K1_MS = 150528;
constexpr int K1_C1 = 154624;
constexpr int K1_LDS = 156672;

__global__ __launch_bounds__(512, 2) void k1(const unsigned short* __restrict__ x, const unsigned short* __restrict__ wqkv,
                                             const unsigned short* __restrict__ wphi, const unsigned short* __restrict__ wo,
                                             const unsigned short* __restrict__ w1, const unsigned short* __restrict__ w2, char* __restrict__ ws) {
  extern __shared__ __attribute__((aligned(16))) char sm_[];
  LAS char* sm = (LAS char*)sm_;
  const unsigned smb = (unsigned)(size_t)sm_;
  const int lane = threadIdx.x & 63, wv = __builtin_amdgcn_readfirstlane(threadIdx.x >> 6);
  const int tg = wv & 3, KH = wv >> 2;
  const int r = lane & 31, hh = lane >> 5;
  const int b = blockIdx.x, xcd = b & 7, bi = b >> 3;
  const int tt = 4 * (xcd >> 1) + (bi & 3), g = 8 * (xcd & 1) + (bi >> 2);
  const int row8 = lane >> 3, part = lane & 7, p4 = lane & 3;
  const __amdgpu_buffer_rsrc_t rs = __builtin_amdgcn_make_buffer_rsrc((void*)ws, 0, WS_BYTES, 0x00020000);
  const int l16 = lane * 16;
  STAMP_DECL; STAMP(0);
  v4u xr[16];
  const unsigned xvo = row8 * 1024 + part * 16;
  const char* xsb = (const char*)x + (size_t)(tt * 128 + tg * 32) * 1024 + KH * 512;
  const unsigned wvo = (lane >> 2) * 3072 + p4 * 16;
  const char* wsb = (const char*)wqkv + (size_t)(256 * KH + 16 * tg) * 3072 + g * 64;
  const unsigned wld = smb + K1_WL + ((16 * KH + tg) * 3) * 1024;
#define XLOAD1(R, rg) asm volatile("global_load_dwordx4 %0, %1, %2 offset:%3" : "=v"(xr[4 * (R) + (rg)]) : "v"(xvo), "s"(xsb + (rg) * 8192), "n"((R) * 128) : "memory")
#define XLOAD(R) do { XLOAD1(R, 0); XLOAD1(R, 1); XLOAD1(R, 2); XLOAD1(R, 3); } while (0)
#define WLOAD(R) glds3(wvo, wsb + (size_t)(R) * 64 * 3072, wld + (R) * 12288)
  XLOAD(0); WLOAD(0); XLOAD(1);
  STAMP(1);
  const bool isw1 = b < 86;
  int rt_dst[2];
  f32x16 aq = zero16(), ak = zero16(), av = zero16(), c0 = zero16(), c1 = zero16(), c2 = zero16();
  const v4u ones = ONES4;
  float sa = 0.f, sb = 0.f, qa = 0.f, qb = 0.f;
  const int bl = (lane >> 4) & 1, q4 = (lane & 15) >> 2;
  LAS char* ap0 = sm + K1_WL + (16 * KH) * 3072 + (8 * hh + q4) * 64 + (bl ^ KH) * 32 + p4 * 8;
  LAS char* ap[4] = {ap0 + tg * 3072, ap0 + (tg ^ 1) * 3072, ap0 + (tg ^ 2) * 3072, ap0 + (tg ^ 3) * 3072};
  LAS char* stg = sm + K1_WV + wv * 6144;
  LAS char* stw0 = stg + row8 * 128 + ((part ^ (row8 >> 1)) << 4);
  LAS char* stw1 = stg + row8 * 128 + ((part ^ (row8 >> 1) ^ 4) << 4);
  const unsigned srd = (r * 128 + ((hh ^ ((r >> 1) & 7)) << 4)) ^ (tg << 5);
#define XWAIT(R, N) asm volatile("s_waitcnt vmcnt(%4)" : "+v"(xr[4 * (R)]), "+v"(xr[4 * (R) + 1]), "+v"(xr[4 * (R) + 2]), "+v"(xr[4 * (R) + 3]) : "n"(N) : "memory")
#define STW(R, k) *(LAS v4u*)(((k) & 1 ? stw1 : stw0) + (k) * 1024) = xr[4 * (R) + (k)]
#define STR(XF, k) XF[k] = *(LAS v4u*)(stg + (srd ^ ((k) << 5)))
#define STAT(v) do { const float lo_ = blo(v), hi_ = bhi(v); sa += lo_; sb += hi_; qa = __builtin_fmaf(lo_, lo_, qa); qb = __builtin_fmaf(hi_, hi_, qb); \
    asm volatile("" : "+v"(sa), "+v"(sb), "+v"(qa), "+v"(qb)); } while (0)
  v4u xf[2][4];
  XWAIT(0, 7);
  STW(0, 0); STW(0, 1); STW(0, 2); STW(0, 3); STR(xf[0], 0); STR(xf[0], 1); STR(xf[0], 2); STR(xf[0], 3);
  asm volatile("s_waitcnt vmcnt(4)" ::: "memory");
#pragma unroll
  for (int rho = 0; rho < 4; ++rho) {
    BAR();
    STAMP(2 + rho);
    v4u f[2][3];
#pragma unroll
    for (int c = 0; c < 3; ++c) f[0][c] = tr2(ap[0] + rho * 12288 + c * 1024, 256);
    SB();
#pragma unroll
    for (int u = 0; u < 4; ++u) {
      const v4u xb = xf[rho & 1][u];
#pragma unroll
      for (int c = 0; c < 3; ++c) {
        if (c == 0) aq = mfma(f[u & 1][0], xb, aq);
        if (c == 1) ak = mfma(f[u & 1][1], xb, ak);
        if (c == 2) av = mfma(f[u & 1][2], xb, av);
        if (u < 3) f[(u + 1) & 1][c] = tr2(ap[u + 1] + rho * 12288 + c * 1024, 256);
        STAT(xb[c]);
        if (rho < 3 && u == 0 && c == 0) WLOAD(rho + 1);
        if (rho < 2 && u == 0 && c == 1) { XLOAD1(rho + 2, 0); XLOAD1(rho + 2, 1); }
        if (rho < 2 && u == 0 && c == 2) { XLOAD1(rho + 2, 2); XLOAD1(rho + 2, 3); }
        if (rho == 2 && u == 0 && c == 1) {
#pragma unroll
          for (int u2 = 0; u2 < 2; ++u2) {
            const int i = 2 * wv + u2;
            const char* src; int dst;
            if (isw1) {
              const int T = b >> 1, s = 16 * (b & 1) + i, e = T - 32;
              const int q = T < 32 ? (T & 15) : (4 * (e & 3) + (e >> 2)), t = T < 32 ? (T >> 4) : 2;
              const int p = ((((s >> 3) - (q >> 2)) & 3) << 3) + (s & 7);
              const int m = p < 8 ? 3 * p + t : 16 + 24 * t + p;
              src = (const char*)w1 + (size_t)s * 44032 + T * 64;
              dst = W1T_OFF + (q * 96 + m) * 1024;
            } else {
              int id = (b - 86) * 12 + i;
              const bool ok = (i < 12) && (id < 1888);
              id = ok ? id : 0;
              const int sb = id >> 4, q = id & 15;
              if (sb < 32) { src = (const char*)wo + sb * 16384 + q * 64; dst = WOT_OFF + (q * 32 + sb) * 1024; }
              else { src = (const char*)w2 + (sb - 32) * 16384 + q * 64; dst = W2T_OFF + (q * 86 + pos_c(sb - 32, q >> 2)) * 1024; }
              if (!ok) dst = DUMMY_OFF;
            }
            rt_dst[u2] = dst;
            glds1((lane >> 2) * (isw1 ? 2752 : 1024) + p4 * 16, src, smb + K1_WV + wv * 6144 + 4096 + u2 * 1024);
          }
        }
        if (rho < 3 && u == 2 && c == 0) { XWAIT(rho + 1, rho == 2 ? 5 : 7); STW(rho + 1, 0); }
        if (rho < 3 && u == 2 && c == 1) { STW(rho + 1, 1); }
        if (rho < 3 && u == 2 && c == 2) { STW(rho + 1, 2); }
        if (rho < 3 && u == 3 && c == 0) { STW(rho + 1, 3); }
        if (rho < 3 && u == 3 && c == 1) { STR(xf[(rho + 1) & 1], 0); STR(xf[(rho + 1) & 1], 1); }
        if (rho < 3 && u == 3 && c == 2) { STR(xf[(rho + 1) & 1], 2); STR(xf[(rho + 1) & 1], 3); }
        SB();
        if (u == 0) {
          if (c == 0) c0 = mfma(ones, f[0][0], c0);
          if (c == 1) c1 = mfma(ones, f[0][1], c1);
          if (c == 2) c2 = mfma(ones, f[0][2], c2);
          if (c == 2) STAT(xb[3]);
          SB();
        }
      }
      if (u > 0) { STAT(xb[3]); SB(); }
    }
    if (rho < 3) asm volatile("s_waitcnt vmcnt(%0)" :: "n"(rho == 2 ? 2 : 4) : "memory");
  }
  STAMP(6);
  unsigned we[8];
  {
    asm volatile("s_waitcnt vmcnt(0)" ::: "memory");
    LAS char* ra = stg + 4096 + (4 * hh + q4) * 64 + p4 * 8;
    const v4u f0 = tr2(ra + bl * 32, 512), f1 = tr2(ra + 1024 + bl * 32, 512);
    __builtin_amdgcn_raw_buffer_store_b128(f0, rs, l16, rt_dst[0], 0);
    __builtin_amdgcn_raw_buffer_store_b128(f1, rs, l16, rt_dst[1], 0);
#pragma unroll
    for (int j = 0; j < 8; ++j) we[j] = wphi[((2 * g + KH) * 16 + 8 * (j >> 2) + 4 * hh + (j & 3)) * 16 + (r & 15)];
    if (isw1) {
      f32x16 ca = mfma(ones, f0, zero16());
      ca = mfma(ones, f1, ca);
      if (hh == 0) ((LAS float*)(sm + K1_C1))[wv * 32 + r] = ca[0];
    }
  }
  *(LAS f32x2*)(sm + K1_MS + (tg * 32 + r) * 32 + KH * 16 + hh * 8) = f32x2{sa + sb, qa + qb};
  if (hh == 0) { LAS float* cs = (LAS float*)(sm + K1_CS) + (r ^ (16 * KH)) * 8 + wv; cs[0] = c0[0]; cs[256] = c1[0]; cs[512] = c2[0]; }
  {
    LAS f32x4* ex = (LAS f32x4*)(stg) + lane;
    ex[0] = f32x4{aq[8], aq[9], aq[10], aq[11]}; ex[64] = f32x4{aq[12], aq[13], aq[14], aq[15]};
    ex[128] = f32x4{ak[8], ak[9], ak[10], ak[11]}; ex[192] = f32x4{ak[12], ak[13], ak[14], ak[15]};
    ex[256] = f32x4{av[8], av[9], av[10], av[11]}; ex[320] = f32x4{av[12], av[13], av[14], av[15]};
  }
  LGKM0();
  BAR();
  STAMP(7);
  float rstd;
  {
    LAS f32x4* ex = (LAS f32x4*)(sm + K1_WV + (wv ^ 4) * 6144) + lane;
    LAS f32x4* sp = (LAS f32x4*)(sm + K1_MS + (tg * 32 + r) * 32);
    LAS f32x4* cp = (LAS f32x4*)(sm + K1_CS + (r ^ (16 * KH)) * 32);
    const f32x4 st = sp[0] + sp[1];
    const f32x4 cb = cp[64] + cp[65];
    const f32x4 e2 = ex[128], e3 = ex[192];
    const float mean = (st[0] + st[2]) * (1.0f / 512.0f);
    const float hz = hh ? 0.f : 1.f;
    const float cs1 = ((cb[0] + cb[1]) + (cb[2] + cb[3])) * hz;
#pragma unroll
    for (int e = 0; e < 4; ++e) { ak[e] += e2[e]; ak[4 + e] += e3[e]; }
    ak = mfma32(cs1, -mean, ak);
    const f32x4 ca = cp[0] + cp[1], cc = cp[128] + cp[129];
    const f32x4 e0 = ex[0], e1 = ex[64], e4 = ex[256], e5 = ex[320];
    const float cs0 = ((ca[0] + ca[1]) + (ca[2] + ca[3])) * hz, cs2 = ((cc[0] + cc[1]) + (cc[2] + cc[3])) * hz;
#pragma unroll
    for (int e = 0; e < 4; ++e) { aq[e] += e0[e]; aq[4 + e] += e1[e]; av[e] += e4[e]; av[4 + e] += e5[e]; }
    const float var = vmax((st[1] + st[3]) * (1.0f / 512.0f) - mean * mean, 0.f);
    rstd = __builtin_amdgcn_rsqf(var + 1e-5f);
    av = mfma32(cs2, -mean, av);
    aq = mfma32(cs0, -mean, aq);
  }
  const unsigned sg = (r >= 16) ? 0x80008000u : 0u;
  const v4u wp = v4u{(we[0] | (we[1] << 16)) ^ sg, (we[2] | (we[3] << 16)) ^ sg, (we[4] | (we[5] << 16)) ^ sg, (we[6] | (we[7] << 16)) ^ sg};
  f32x16 pk = mfma(wp, pack8(ak, 0), zero16());
  {
    LAS char* vi = sm + K1_V + tg * 2048 + r * 64 + KH * 32 + hh * 8;
    *(LAS v2u*)(vi) = v2u{pk2(av[0] * rstd, av[1] * rstd), pk2(av[2] * rstd, av[3] * rstd)};
    *(LAS v2u*)(vi + 16) = v2u{pk2(av[4] * rstd, av[5] * rstd), pk2(av[6] * rstd, av[7] * rstd)};
  }
  dual_softmax_sc(pk, rstd * 1.4426950408889634f);
  {
    LAS char* pi = sm + K1_PK + (tg * 2 + KH) * 2048 + r * 64 + hh * 8;
#pragma unroll
    for (int gq = 0; gq < 4; ++gq) *(LAS v2u*)(pi + gq * 16) = v2u{pk2(pk[4 * gq], pk[4 * gq + 1]), pk2(pk[4 * gq + 2], pk[4 * gq + 3])};
  }
  LGKM0();
  BAR();
  STAMP(8);
  if (tg == 0) {
    f32x16 z = zero16();
    const bool isv = ((r >> 4) == KH);
    LAS char* pa = sm + K1_PK + KH * 2048 + (8 * hh + q4) * 64 + bl * 32 + p4 * 8;
    LAS char* pv = sm + K1_V + (8 * hh + q4) * 64 + bl * 32 + p4 * 8;
#pragma unroll
    for (int ks = 0; ks < 8; ++ks) {
      const v4u a = tr2(pa + (ks >> 1) * 4096 + (ks & 1) * 1024, 256);
      v4u bv = tr2(pv + ks * 1024, 256);
      if (!isv) bv = ones;
      z = mfma(a, bv, z);
    }
    if (isv || ((r & 15) == 0)) {
      GAS f32x4* dst = (GAS f32x4*)(ws + KVP_OFF + (size_t)(tt * 32 + 2 * g + KH) * 2176 + (isv ? (r & 15) : 16) * 128 + hh * 64);
#pragma unroll
      for (int gq = 0; gq < 4; ++gq) dst[gq] = f32x4{z[4 * gq], z[4 * gq + 1], z[4 * gq + 2], z[4 * gq + 3]};
    }
  } else if (tg == 1) {
    if (KH == 0 && isw1 && lane < 32) {
      LAS float* cp = (LAS float*)(sm + K1_C1) + lane;
      float s = 0.f;
#pragma unroll
      for (int k = 0; k < 8; ++k) s += cp[32 * k];
      ((GAS float*)(ws + CS1_OFF))[b * 32 + lane] = s;
    }
    if (KH == 1) { __builtin_amdgcn_raw_buffer_store_b128(v4u{SENT, SENT, SENT, SENT}, rs, l16, KVF_OFF + b * 1024, 0); if (b < 32) __builtin_amdgcn_raw_buffer_store_b128(v4u{SENT, SENT, SENT, SENT}, rs, l16, KVF_OFF + (256 + b) * 1024, 0); }
    if (KH == 1 && lane < 16) ((gu32*)(ws + FLG_OFF))[(b * 16 + lane) * 16] = 0u;
  }
  {
    f32x16 pq = mfma(wp, pack8(aq, 0), zero16());
    dual_softmax_sc(pq, rstd * 1.4426950408889634f);
    const int qo = QF_OFF + ((tt * 4 + tg) * 32 + 2 * g + KH) * 2048;
    __builtin_amdgcn_raw_buffer_store_b128(pack8(pq, 0), rs, l16, qo, 0);
    __builtin_amdgcn_raw_buffer_store_b128(pack8(pq, 1), rs, l16, qo + 1024, 0);
  }
  STAMP(9); STAMP_FLUSH(0);
}

constexpr int K2_OF = 0;
constexpr int K2_HF = 32768;
constexpr int K2_H1 = 65536;
constexpr int K2_ST = 153600;
constexpr int K2_LDS = 154624;
#define KV_GATE 190
constexpr int RDA = 42, RDB = 38;
constexpr int OOB = 0x7FFFFF00;

DI void wait_flags(gu32* f, int j, int lane) {
  const bool need = lane < 4 && lane != j;
#pragma unroll 1
  for (unsigned sp = 0; sp < (1u << 22); ++sp) {
    unsigned v = 1u;
    if (need) v = __hip_atomic_load(f + lane * 16, __ATOMIC_RELAXED, __HIP_MEMORY_SCOPE_AGENT);
    if (__builtin_amdgcn_ballot_w64(v == 0u) == 0ull) break;
    __builtin_amdgcn_s_sleep(1);
  }
  __builtin_amdgcn_fence(__ATOMIC_ACQUIRE, "wavefront");
}
#define VM0() asm volatile("s_waitcnt vmcnt(0)" ::: "memory")
#define SETFLAG(p) do { if (lane == 0) __hip_atomic_store((p), 1u, __ATOMIC_RELAXED, __HIP_MEMORY_SCOPE_AGENT); } while (0)

DI void o_phase(const v4u (&pf)[4][2], __amdgpu_buffer_rsrc_t rs, LAS char* sm, int b, int wv, int lane, unsigned long long tstart) {
  const int r = lane & 31, hh = lane >> 5;
  while (__builtin_amdgcn_s_memrealtime() - tstart < KV_GATE) __builtin_amdgcn_s_sleep(1);
  v4u kv[4][2];
  const int ko = (r < 16) ? r * 64 + hh * 32 : ((r == 16) | (r == 20)) ? 1024 + hh * 32 : OOB;
#pragma unroll 1
  for (unsigned sp = 0; sp < (1u << 22); ++sp) {
    bool bad = false;
#pragma unroll
    for (int hq = 0; hq < 4; ++hq)
#pragma unroll
      for (int s = 0; s < 2; ++s) {
        kv[hq][s] = __builtin_amdgcn_raw_buffer_load_b128(rs, ko + s * 16, KVF_OFF + (b & 7) * KVF_REP + (4 * wv + hq) * 1088, 16);
        bad |= hassent(kv[hq][s]);
      }
    if (__builtin_amdgcn_ballot_w64(bad) == 0ull) break;
    __builtin_amdgcn_s_sleep(4);
  }
#pragma unroll
  for (int hq = 0; hq < 4; ++hq) {
    f32x16 d = mfma(kv[hq][0], pf[hq][0], zero16());
    d = mfma(kv[hq][1], pf[hq][1], d);
    const float inv = __builtin_amdgcn_rcpf(d[8] + 1e-6f);
#pragma unroll
    for (int i = 0; i < 8; ++i) d[i] *= inv;
    *(LAS v4u*)(sm + K2_OF + (4 * wv + hq) * 1024 + lane * 16) = pack8(d, 0);
  }
}

__global__ __launch_bounds__(512, 2) void k2(const unsigned short* __restrict__ x, const unsigned short* __restrict__ b1,
                                             const unsigned short* __restrict__ b2, char* __restrict__ ws, unsigned short* __restrict__ out) {
  extern __shared__ __attribute__((aligned(16))) char sm_[];
  LAS char* sm = (LAS char*)sm_;
  const unsigned smb = (unsigned)(size_t)sm_;
  const int lane = threadIdx.x & 63, wv = __builtin_amdgcn_readfirstlane(threadIdx.x >> 6);
  const int w = wv & 3;
  const int r = lane & 31, hh = lane >> 5;
  const int b = blockIdx.x, c = (b & 7) * 8 + (b >> 5), j = (b >> 3) & 3, q = 4 * j + w;
  const bool three = (4 * w + j) < 11;
  const __amdgpu_buffer_rsrc_t rs = __builtin_amdgcn_make_buffer_rsrc((void*)ws, 0, WS_BYTES, 0x00020000);
  const int l16 = lane * 16;
  const unsigned long long tstart = __builtin_amdgcn_s_memrealtime();
  STAMP_DECL; STAMP(0);
  LAS char* hf = sm + K2_HF + l16;
  LAS char* h1 = sm + K2_H1 + l16;
  if (wv < 4) {
    constexpr int RD = RDA, NTOT = 118;
    const bool red = b < 136;
    f32x4 kp[2] = {{0.f, 0.f, 0.f, 0.f}, {0.f, 0.f, 0.f, 0.f}};
    if (red) {
      const GAS f32x4* pp = (const GAS f32x4*)(ws + KVP_OFF + (size_t)(lane >> 2) * 69632) + (b * 4 + w) * 8 + (lane & 3);
      kp[0] = pp[0]; kp[1] = pp[4];
    }
    v2u xr[4];
#pragma unroll
    for (int g = 0; g < 4; ++g) xr[g] = *(const GAS v2u*)((const char*)x + (size_t)(32 * c + r) * 1024 + (32 * q + 8 * g + 4 * hh) * 2);
    const unsigned bb2 = b2[32 * q + r];
    v4u pf[4][2];
#pragma unroll
    for (int hq = 0; hq < 4; ++hq)
#pragma unroll
      for (int s = 0; s < 2; ++s) pf[hq][s] = __builtin_amdgcn_raw_buffer_load_b128(rs, l16, QF_OFF + ((c * 32 + 4 * wv + hq) * 2 + s) * 1024, 0);
    v4u ring[RD];
    const int sA = WOT_OFF + q * 32768, sC = W2T_OFF + q * 88064 - 32768;
#define RLOAD(i) do { if ((i) < NTOT) ring[(i) % RD] = __builtin_amdgcn_raw_buffer_load_b128(rs, l16, ((i) < 32 ? sA : sC) + (i) * 1024, 0); } while (0)
#define RLOADS(lo, hi) _Pragma("unroll") for (int i_ = (lo); i_ < (hi); ++i_) RLOAD(i_)
    RLOADS(0, 8);
    SB();
    if (red) {
#pragma unroll
      for (int k = 0; k < 2; ++k) {
#pragma unroll
        for (int e = 0; e < 4; ++e) {
          float v = kp[k][e];
          v += __builtin_bit_cast(float, __builtin_amdgcn_update_dpp(0, __builtin_bit_cast(int, v), 0x124, 0xf, 0xf, false));
          v += __builtin_bit_cast(float, __builtin_amdgcn_update_dpp(0, __builtin_bit_cast(int, v), 0x128, 0xf, 0xf, false));
          { auto s2 = __builtin_amdgcn_permlane16_swap(__float_as_uint(v), __float_as_uint(v), false, false); v = __uint_as_float(s2[0]) + __uint_as_float(s2[1]); }
          kp[k][e] = xsum(v);
        }
      }
      const f32x4 kk = (lane & 4) ? kp[1] : kp[0];
      unsigned v0 = pk2(kk[0], kk[1]), v1 = pk2(kk[2], kk[3]);
      v0 = (v0 == SENT) ? 0xFFFEFFFEu : v0; v1 = (v1 == SENT) ? 0xFFFEFFFEu : v1;
      __hip_atomic_store((GAS unsigned long long*)(ws + KVF_OFF + (lane >> 3) * KVF_REP) + (b * 4 + w) * 8 + (lane & 7), (unsigned long long)v0 | ((unsigned long long)v1 << 32), __ATOMIC_RELAXED, __HIP_MEMORY_SCOPE_AGENT);
    }
    RLOADS(8, 20);
    SB();
    STAMP(1);
    o_phase(pf, rs, sm, b, wv, lane, tstart);
    RLOADS(20, 32);
    LGKM0();
    BAR();
    STAMP(2);
    v4u hf0, hf1;
    {
      f32x16 acc;
#pragma unroll
      for (int g = 0; g < 4; ++g) { acc[4 * g] = blo(xr[g][0]); acc[4 * g + 1] = bhi(xr[g][0]); acc[4 * g + 2] = blo(xr[g][1]); acc[4 * g + 3] = bhi(xr[g][1]); }
      LAS char* ob = sm + K2_OF + l16;
      v4u bq[4];
#pragma unroll
      for (int i = 0; i < 3; ++i) bq[i] = *(LAS v4u*)(ob + i * 1024);
#pragma unroll
      for (int i = 0; i < 32; ++i) { if (i + 3 < 32) bq[(i + 3) & 3] = *(LAS v4u*)(ob + (i + 3) * 1024); acc = mfma(ring[i % RD], bq[i & 3], acc); SB(); }
      hf0 = pack8(acc, 0); hf1 = pack8(acc, 1);
    }
    __builtin_amdgcn_raw_buffer_store_b128(hf0, rs, l16, HX_OFF + (c * 32 + 2 * q) * 1024, 16);
    __builtin_amdgcn_raw_buffer_store_b128(hf1, rs, l16, HX_OFF + (c * 32 + 2 * q + 1) * 1024, 16);
    *(LAS v4u*)(hf + (2 * w) * 1024) = hf0;
    *(LAS v4u*)(hf + (2 * w + 1) * 1024) = hf1;
    LGKM0();
    BAR();
    gu32* fl = (gu32*)(ws + FLG_OFF) + ((c * 4 + w) * 4) * 16;
    VM0();
    SETFLAG(fl + j * 16);
    STAMP(3);
    wait_flags(fl, j, lane);
#pragma unroll
    for (int d = 1; d < 4; ++d)
#pragma unroll
      for (int k = 0; k < 2; ++k) glds1c(l16, ws + HX_OFF + (c * 32 + 2 * (4 * ((j + d) & 3) + w) + k) * 1024, smb + K2_HF + (8 * d + 2 * w + k) * 1024);
    VM0();
    BAR();
    STAMP(4);
    {
      const v4u ones = ONES4;
      f32x16 ag = zero16(), as = zero16();
#pragma unroll
      for (int u = 0; u < 8; ++u) {
        const v4u hb = *(LAS v4u*)(hf + (8 * w + u) * 1024);
        ag = mfma(hb, hb, ag);
        as = mfma(ones, hb, as);
      }
      const float dg = diag16(ag, lane);
      if (hh == 0) *(LAS f32x2*)(sm + K2_ST + r * 32 + w * 8) = f32x2{as[0], dg};
    }
    RLOADS(32, 64);
    f32x16 acc = mfma32(hh ? 0.f : blo(bb2), 1.0f, zero16());
#pragma unroll
    for (int e = 0; e < 4; ++e) { acc[2 * e] += blo(hf0[e]); acc[2 * e + 1] += bhi(hf0[e]); acc[8 + 2 * e] += blo(hf1[e]); acc[9 + 2 * e] += bhi(hf1[e]); }
    LGKM0();
    BAR();
    BAR();
    STAMP(5);
#define CNX(p) ((64 + 2 * (p)) < (32 + (p) + RD) ? (64 + 2 * (p)) : (32 + (p) + RD))
#define CSTEPS(lo, hi) do { v4u bq_[4]; _Pragma("unroll") for (int p_ = (lo); p_ < (lo) + 3; ++p_) bq_[p_ & 3] = *(LAS v4u*)(h1 + p_ * 1024); \
    _Pragma("unroll") for (int p_ = (lo); p_ < (hi); ++p_) { if (p_ + 3 < (hi)) bq_[(p_ + 3) & 3] = *(LAS v4u*)(h1 + (p_ + 3) * 1024); \
      acc = mfma(ring[(32 + p_) % RD], bq_[p_ & 3], acc); RLOADS(CNX(p_), CNX(p_ + 1)); SB(); } } while (0)
#define PUB(T, pos) do { __builtin_amdgcn_raw_buffer_store_b128(*(LAS v4u*)(h1 + (pos) * 1024), rs, l16, H1X_OFF + (c * 86 + 2 * (T)) * 1024, 16); \
    __builtin_amdgcn_raw_buffer_store_b128(*(LAS v4u*)(h1 + ((pos) + 1) * 1024), rs, l16, H1X_OFF + (c * 86 + 2 * (T) + 1) * 1024, 16); } while (0)
#define GATHER(st) _Pragma("unroll") for (int d = 1; d < 4; ++d) _Pragma("unroll") for (int k = 0; k < 2; ++k) \
    glds1c(l16, ws + H1X_OFF + (c * 86 + 2 * (16 * (st) + 4 * ((j + d) & 3) + w) + k) * 1024, smb + K2_H1 + (8 + 24 * (st) + 8 * d + 2 * w + k) * 1024)
    PUB(q, 2 * w);
    VM0();
    SETFLAG(fl + 16384 + j * 16);
    CSTEPS(0, 8);
    BAR();
    STAMP(6);
    PUB(q + 16, 8 + 2 * w);
    wait_flags(fl + 16384, j, lane);
    GATHER(0);
    VM0();
    SETFLAG(fl + 32768 + j * 16);
    CSTEPS(8, 16);
    BAR();
    STAMP(7);
    if (three) PUB(32 + 4 * w + j, 64 + 2 * (4 * w + j));
    VM0();
    SETFLAG(fl + 49152 + j * 16);
    wait_flags(fl + 32768, j, lane);
    GATHER(1);
    CSTEPS(16, 40);
    VM0();
    BAR();
    STAMP(8);
    wait_flags(fl + 49152, j, lane);
#pragma unroll
    for (int d = 1; d < 4; ++d) {
      const int e = 4 * w + ((j + d) & 3);
      if (e < 11) {
        glds1c(l16, ws + H1X_OFF + (c * 86 + 64 + 2 * e) * 1024, smb + K2_H1 + (64 + 2 * e) * 1024);
        glds1c(l16, ws + H1X_OFF + (c * 86 + 65 + 2 * e) * 1024, smb + K2_H1 + (65 + 2 * e) * 1024);
      }
    }
    CSTEPS(40, 64);
    VM0();
    BAR();
    STAMP(9);
    CSTEPS(64, 86);
    STAMP(10);
#pragma unroll
    for (int k = 0; k < 2; ++k) {
      const v4u o = pack8(acc, k);
      auto r0 = __builtin_amdgcn_permlane32_swap(o[0], o[2], false, false);
      auto r1 = __builtin_amdgcn_permlane32_swap(o[1], o[3], false, false);
      *(GAS v4u*)((char*)out + (size_t)(32 * c + r) * 1024 + (32 * q + 16 * k + 8 * hh) * 2) = v4u{r0[0], r1[0], r0[1], r1[1]};
    }
#undef RLOAD
#undef RLOADS
  } else {
    constexpr int RD = RDB, NTOT = 96;
    const int T2 = three ? (32 + 4 * w + j) : q;
    float csa[3], csb[3]; unsigned bb1[3];
#pragma unroll
    for (int t = 0; t < 3; ++t) {
      const int T = t < 2 ? q + 16 * t : T2;
      const GAS float* cp = (const GAS float*)(ws + CS1_OFF) + T * 64 + r;
      csa[t] = cp[0]; csb[t] = cp[32]; bb1[t] = b1[32 * T + r];
    }
    v4u pf[4][2];
#pragma unroll
    for (int hq = 0; hq < 4; ++hq)
#pragma unroll
      for (int s = 0; s < 2; ++s) pf[hq][s] = __builtin_amdgcn_raw_buffer_load_b128(rs, l16, QF_OFF + ((c * 32 + 4 * wv + hq) * 2 + s) * 1024, 0);
    v4u ring[RD];
    const int sB = W1T_OFF + q * 98304;
    const int l3 = three ? l16 : OOB;
#define RLOAD(i) do { if ((i) < NTOT) ring[(i) % RD] = __builtin_amdgcn_raw_buffer_load_b128(rs, ((i) < 24 ? (i) % 3 == 2 : (i) >= 72) ? l3 : l16, sB + (i) * 1024, 0); } while (0)
#define RLOADS(lo, hi) _Pragma("unroll") for (int i_ = (lo); i_ < (hi); ++i_) RLOAD(i_)
    SB();
    STAMP(1);
    o_phase(pf, rs, sm, b, wv, lane, tstart);
    RLOADS(0, 4);
    float ab1[3];
#pragma unroll
    for (int t = 0; t < 3; ++t) ab1[t] = hh ? blo(bb1[t]) : csa[t] + csb[t];
    asm volatile("" : "+v"(ab1[0]), "+v"(ab1[1]), "+v"(ab1[2]));
    LGKM0();
    BAR();
    STAMP(2);
    RLOADS(4, RD);
    BAR();
    STAMP(3);
    f32x16 ac[3];
#pragma unroll
    for (int t = 0; t < 3; ++t) ac[t] = zero16();
    v4u hq2[2];
    hq2[0] = *(LAS v4u*)(hf);
#pragma unroll
    for (int p = 0; p < 8; ++p) {
      const v4u hb = hq2[p & 1];
      if (p < 7) hq2[(p + 1) & 1] = *(LAS v4u*)(hf + (p + 1) * 1024);
#pragma unroll
      for (int t = 0; t < 3; ++t) { ac[t] = mfma(ring[(3 * p + t) % RD], hb, ac[t]); RLOAD(3 * p + t + RD); SB(); }
    }
    BAR();
    STAMP(4);
#define BREST(t) do { v4u bq_[4]; _Pragma("unroll") for (int p_ = 8; p_ < 11; ++p_) bq_[p_ & 3] = *(LAS v4u*)(hf + p_ * 1024); \
    _Pragma("unroll") for (int p_ = 8; p_ < 32; ++p_) { if (p_ + 3 < 32) bq_[(p_ + 3) & 3] = *(LAS v4u*)(hf + (p_ + 3) * 1024); \
      ac[t] = mfma(ring[(16 + 24 * (t) + p_) % RD], bq_[p_ & 3], ac[t]); RLOAD(16 + 24 * (t) + p_ + RD); SB(); } } while (0)
#define FINISH(t, pos_) do { ac[t] = mfma32(ab1[t], bop, ac[t]); \
    _Pragma("unroll") for (int e = 0; e < 16; ++e) { const float p_ = vmax(ac[t][e], 0.f); ac[t][e] = (p_ * p_) * r2; } \
    *(LAS v4u*)(h1 + (pos_) * 1024) = pack8(ac[t], 0); *(LAS v4u*)(h1 + ((pos_) + 1) * 1024) = pack8(ac[t], 1); } while (0)
    BREST(0);
    BAR();
    float bop, r2;
    {
      LAS f32x4* sp = (LAS f32x4*)(sm + K2_ST + r * 32);
      const f32x4 st = sp[0] + sp[1];
      const float mean = (st[0] + st[2]) * (1.0f / 512.0f);
      const float var = vmax((st[1] + st[3]) * (1.0f / 512.0f) - mean * mean, 0.f) + 1e-5f;
      const float rstd = __builtin_amdgcn_rsqf(var);
      bop = hh ? var * rstd : -mean;
      r2 = rstd * rstd;
    }
    FINISH(0, 2 * w);
    LGKM0();
    BAR();
    STAMP(5);
    BREST(1);
    FINISH(1, 8 + 2 * w);
    LGKM0();
    BAR();
    STAMP(6);
    if (three) {
      BREST(2);
      FINISH(2, 64 + 2 * (4 * w + j));
    }
    LGKM0();
    BAR();
    STAMP(7);
    BAR();
    BAR();
#undef RLOAD
#undef RLOADS
  }
  STAMP(11); STAMP_FLUSH(1);
}
}

extern "C" void kernel_launch(void* const* d_in, const int* in_sizes, int n_in, void* d_out, int out_size, void* d_ws, size_t ws_size,
                              hipStream_t stream) {
  const unsigned short* x = (const unsigned short*)d_in[0];
  const unsigned short* wqkv = (const unsigned short*)d_in[1];
  const unsigned short* wphi = (const unsigned short*)d_in[2];
  const unsigned short* wo = (const unsigned short*)d_in[3];
  const unsigned short* w1 = (const unsigned short*)d_in[5];
  const unsigned short* b1 = (const unsigned short*)d_in[6];
  const unsigned short* w2 = (const unsigned short*)d_in[7];
  const unsigned short* b2 = (const unsigned short*)d_in[8];
  char* ws = (char*)d_ws;
  (void)hipFuncSetAttribute((const void*)k1, hipFuncAttributeMaxDynamicSharedMemorySize, K1_LDS);
  (void)hipFuncSetAttribute((const void*)k2, hipFuncAttributeMaxDynamicSharedMemorySize, K2_LDS);
  k1<<<256, 512, K1_LDS, stream>>>(x, wqkv, wphi, wo, w1, w2, ws);
  k2<<<256, 512, K2_LDS, stream>>>(x, b1, b2, ws, (unsigned short*)d_out);
}
```

```cpp
#include <hip/hip_runtime.h>
#include <hip/hip_bf16.h>
#include <stdint.h>

#define GAS __attribute__((address_space(1)))
#define LAS __attribute__((address_space(3)))
#define DI __device__ __forceinline__
typedef unsigned v4u __attribute__((ext_vector_type(4)));
typedef unsigned v2u __attribute__((ext_vector_type(2)));
typedef float f32x16 __attribute__((ext_vector_type(16)));
typedef float f32x4 __attribute__((ext_vector_type(4)));
typedef float f32x2 __attribute__((ext_vector_type(2)));
typedef __bf16 bf16x8 __attribute__((ext_vector_type(8)));
typedef __bf16 bf16x2 __attribute__((ext_vector_type(2)));
typedef short s16x4 __attribute__((ext_vector_type(4)));
typedef GAS unsigned gu32;

#define STAMP_DECL
#define STAMP(i)
#define STAMP_FLUSH(k)

namespace {
constexpr int WOT_OFF = 0;
constexpr int W1T_OFF = 512 << 10;
constexpr int W2T_OFF = 2048 << 10;
constexpr int QF_OFF = 16 << 20;
constexpr int KVP_OFF = 6 << 20;
constexpr int KVF_OFF = (7 << 20) + (128 << 10);
constexpr int KVF_REP = 36864;
constexpr int CS1_OFF = (7 << 20) + (640 << 10);
constexpr int WPF_OFF = (7 << 20) + (704 << 10);
constexpr int DUMMY_OFF = (7 << 20) + (768 << 10);
constexpr int HX_OFF = 8 << 20;
constexpr int H1X_OFF = 10 << 20;
constexpr int FLG_OFF = (15 << 20) + (512 << 10);
constexpr int WS_BYTES = 20 << 20;
constexpr unsigned SENT = 0xFFFFFFFFu;

DI f32x16 mfma(v4u a, v4u b, f32x16 c) {
  return __builtin_amdgcn_mfma_f32_32x32x16_bf16(__builtin_bit_cast(bf16x8, a), __builtin_bit_cast(bf16x8, b), c, 0, 0, 0);
}
DI f32x16 mfma32(float a, float b, f32x16 c) { return __builtin_amdgcn_mfma_f32_32x32x2f32(a, b, c, 0, 0, 0); }
DI unsigned pk2(float a, float b) { bf16x2 v; v[0] = (__bf16)a; v[1] = (__bf16)b; return __builtin_bit_cast(unsigned, v); }
DI float blo(unsigned v) { return __uint_as_float(v << 16); }
DI float bhi(unsigned v) { return __uint_as_float(v & 0xffff0000u); }
DI v4u pack8(const f32x16& x, int s) {
  return v4u{pk2(x[8 * s + 0], x[8 * s + 1]), pk2(x[8 * s + 2], x[8 * s + 3]), pk2(x[8 * s + 4], x[8 * s + 5]), pk2(x[8 * s + 6], x[8 * s + 7])};
}
DI v4u nosent(v4u v) {
#pragma unroll
  for (int i = 0; i < 4; ++i) v[i] = (v[i] == SENT) ? 0xFFFEFFFEu : v[i];
  return v;
}
DI bool hassent(v4u v) { return (v[0] == SENT) | (v[1] == SENT) | (v[2] == SENT) | (v[3] == SENT); }
DI float xsum(float v) { auto r = __builtin_amdgcn_permlane32_swap(__float_as_uint(v), __float_as_uint(v), false, false); return __uint_as_float(r[0]) + __uint_as_float(r[1]); }
DI float vmax(float a, float b) { return __builtin_elementwise_maximum(a, b); }
DI v2u trread(LAS char* p) { return __builtin_bit_cast(v2u, __builtin_amdgcn_ds_read_tr16_b64_v4i16((LAS s16x4*)p)); }
DI v4u tr2(LAS char* a, int d) { v2u l = trread(a), h = trread(a + d); return v4u{l[0], l[1], h[0], h[1]}; }
DI f32x16 zero16() { f32x16 z; for (int i = 0; i < 16; ++i) z[i] = 0.f; return z; }
DI float diag16(const f32x16& d, int lane) {
  const int r = lane & 31, hh = lane >> 5;
  const int istar = (r & 3) + 4 * (r >> 3);
  float v = 0.f;
#pragma unroll
  for (int i = 0; i < 16; ++i) v = (i == istar) ? d[i] : v;
  v = (hh == ((r >> 2) & 1)) ? v : 0.f;
  return xsum(v);
}
DI void dual_softmax_sc(f32x16& p, float sc) {
#pragma unroll
  for (int hf = 0; hf < 2; ++hf) {
    float m = vmax(vmax(vmax(p[8 * hf], p[8 * hf + 1]), vmax(p[8 * hf + 2], p[8 * hf + 3])), vmax(vmax(p[8 * hf + 4], p[8 * hf + 5]), vmax(p[8 * hf + 6], p[8 * hf + 7])));
    { auto rr = __builtin_amdgcn_permlane32_swap(__float_as_uint(m), __float_as_uint(m), false, false); m = vmax(__uint_as_float(rr[0]), __uint_as_float(rr[1])); }
    m = -(m * sc);
    float s = 0.f;
#pragma unroll
    for (int i = 0; i < 8; ++i) { float e = __builtin_amdgcn_exp2f(__builtin_fmaf(p[8 * hf + i], sc, m)); p[8 * hf + i] = e; s += e; }
    s = xsum(s);
    const float inv = __builtin_amdgcn_rcpf(s);
#pragma unroll
    for (int i = 0; i < 8; ++i) p[8 * hf + i] *= inv;
  }
}
DI void glds3(unsigned voff, const void* sbase, unsigned lds_dst) {
  unsigned keep;
  asm volatile("s_mov_b32 %0, m0\n\ts_mov_b32 m0, %3\n\ts_nop 0\n\tglobal_load_lds_dwordx4 %1, %2\n\tglobal_load_lds_dwordx4 %1, %2 offset:1024\n\t"
               "global_load_lds_dwordx4 %1, %2 offset:2048\n\ts_mov_b32 m0, %0" : "=&s"(keep) : "v"(voff), "s"(sbase), "s"(lds_dst) : "memory");
}
DI void glds1(unsigned voff, const void* sbase, unsigned lds_dst) {
  unsigned keep;
  asm volatile("s_mov_b32 %0, m0\n\ts_mov_b32 m0, %3\n\ts_nop 0\n\tglobal_load_lds_dwordx4 %1, %2\n\ts_mov_b32 m0, %0" : "=&s"(keep) : "v"(voff), "s"(sbase), "s"(lds_dst) : "memory");
}
DI void glds1c(unsigned voff, const void* sbase, unsigned lds_dst) {
  unsigned keep;
  asm volatile("s_mov_b32 %0, m0\n\ts_mov_b32 m0, %3\n\ts_nop 0\n\tglobal_load_lds_dwordx4 %1, %2 sc1\n\ts_mov_b32 m0, %0" : "=&s"(keep) : "v"(voff), "s"(sbase), "s"(lds_dst) : "memory");
}
#define SB() __builtin_amdgcn_sched_barrier(0)
#define LGKM0() asm volatile("s_waitcnt lgkmcnt(0)" ::: "memory")
#define BAR() __builtin_amdgcn_s_barrier()
#define ONES4 v4u{0x3f803f80u, 0x3f803f80u, 0x3f803f80u, 0x3f803f80u}
DI int pos_c(int s, int j) {
  const int T = s >> 1;
  if (T >= 32) return s;
  const int st = T >> 4, d = (((T >> 2) & 3) - j) & 3, lo = 2 * (T & 3) + (s & 1);
  return d == 0 ? 8 * st + lo : 8 + 24 * st + 8 * d + lo;
}

constexpr int K1_WL = 0;
constexpr int K1_PK = 0;
constexpr int K1_V = 16384;
constexpr int K1_WV = 98304;
constexpr int K1_CS = 147456;
constexpr int K1_MS = 150528;
constexpr int K1_C1 = 154624;
constexpr int K1_LDS = 156672;
constexpr unsigned CODE_SPAN = 25000;

__global__ __launch_bounds__(512, 2) void k1(const unsigned short* __restrict__ x, const unsigned short* __restrict__ wqkv,
                                             const unsigned short* __restrict__ wphi, const unsigned short* __restrict__ wo,
                                             const unsigned short* __restrict__ w1, const unsigned short* __restrict__ w2, char* __restrict__ ws) {
  extern __shared__ __attribute__((aligned(16))) char sm_[];
  LAS char* sm = (LAS char*)sm_;
  const unsigned smb = (unsigned)(size_t)sm_;
  const int lane = threadIdx.x & 63, wv = __builtin_amdgcn_readfirstlane(threadIdx.x >> 6);
  const int tg = wv & 3, KH = wv >> 2;
  const int r = lane & 31, hh = lane >> 5;
  const int b = blockIdx.x, xcd = b & 7, bi = b >> 3;
  const int tt = 4 * (xcd >> 1) + (bi & 3), g = 8 * (xcd & 1) + (bi >> 2);
  const int row8 = lane >> 3, part = lane & 7, p4 = lane & 3;
  const __amdgpu_buffer_rsrc_t rs = __builtin_amdgcn_make_buffer_rsrc((void*)ws, 0, WS_BYTES, 0x00020000);
  const int l16 = lane * 16;
  STAMP_DECL; STAMP(0);
  v4u xr[16];
  const unsigned xvo = row8 * 1024 + part * 16;
  const char* xsb = (const char*)x + (size_t)(tt * 128 + tg * 32) * 1024 + KH * 512;
  const unsigned wvo = (lane >> 2) * 3072 + p4 * 16;
  const char* wsb = (const char*)wqkv + (size_t)(256 * KH + 16 * tg) * 3072 + g * 64;
  const unsigned wld = smb + K1_WL + ((16 * KH + tg) * 3) * 1024;
#define XLOAD1(R, rg) asm volatile("global_load_dwordx4 %0, %1, %2 offset:%3" : "=v"(xr[4 * (R) + (rg)]) : "v"(xvo), "s"(xsb + (rg) * 8192), "n"((R) * 128) : "memory")
#define XLOAD(R) do { XLOAD1(R, 0); XLOAD1(R, 1); XLOAD1(R, 2); XLOAD1(R, 3); } while (0)
#define WLOAD(R) glds3(wvo, wsb + (size_t)(R) * 64 * 3072, wld + (R) * 12288)
  XLOAD(0); WLOAD(0); XLOAD(1);
  {
    const unsigned long long pc = __builtin_amdgcn_s_getpc() & ~255ull;
    unsigned off = (unsigned)threadIdx.x * 64u;
    off = off < CODE_SPAN ? off : CODE_SPAN;
    unsigned keep;
    asm volatile("s_mov_b32 %0, m0\n\ts_mov_b32 m0, %3\n\ts_nop 0\n\tglobal_load_lds_dword %1, %2\n\ts_mov_b32 m0, %0" : "=&s"(keep) : "v"(off), "s"(pc), "s"(smb + K1_C1 + wv * 256) : "memory");
  }
  STAMP(1);
  const bool isw1 = b < 86;
  int rt_dst[2];
  f32x16 aq = zero16(), ak = zero16(), av = zero16(), c0 = zero16(), c1 = zero16(), c2 = zero16();
  const v4u ones = ONES4;
  float sa = 0.f, sb = 0.f, qa = 0.f, qb = 0.f;
  const int bl = (lane >> 4) & 1, q4 = (lane & 15) >> 2;
  LAS char* ap0 = sm + K1_WL + (16 * KH) * 3072 + (8 * hh + q4) * 64 + (bl ^ KH) * 32 + p4 * 8;
  LAS char* ap[4] = {ap0 + tg * 3072, ap0 + (tg ^ 1) * 3072, ap0 + (tg ^ 2) * 3072, ap0 + (tg ^ 3) * 3072};
  LAS char* stg = sm + K1_WV + wv * 6144;
  LAS char* stw0 = stg + row8 * 128 + ((part ^ (row8 >> 1)) << 4);
  LAS char* stw1 = stg + row8 * 128 + ((part ^ (row8 >> 1) ^ 4) << 4);
  const unsigned srd = (r * 128 + ((hh ^ ((r >> 1) & 7)) << 4)) ^ (tg << 5);
#define XWAIT(R, N) asm volatile("s_waitcnt vmcnt(%4)" : "+v"(xr[4 * (R)]), "+v"(xr[4 * (R) + 1]), "+v"(xr[4 * (R) + 2]), "+v"(xr[4 * (R) + 3]) : "n"(N) : "memory")
#define STW(R, k) *(LAS v4u*)(((k) & 1 ? stw1 : stw0) + (k) * 1024) = xr[4 * (R) + (k)]
#define STR(XF, k) XF[k] = *(LAS v4u*)(stg + (srd ^ ((k) << 5)))
#define STAT(v) do { const float lo_ = blo(v), hi_ = bhi(v); sa += lo_; sb += hi_; qa = __builtin_fmaf(lo_, lo_, qa); qb = __builtin_fmaf(hi_, hi_, qb); \
    asm volatile("" : "+v"(sa), "+v"(sb), "+v"(qa), "+v"(qb)); } while (0)
  v4u xf[2][4];
  XWAIT(0, 8);
  STW(0, 0); STW(0, 1); STW(0, 2); STW(0, 3); STR(xf[0], 0); STR(xf[0], 1); STR(xf[0], 2); STR(xf[0], 3);
  asm volatile("s_waitcnt vmcnt(5)" ::: "memory");
#pragma unroll
  for (int rho = 0; rho < 4; ++rho) {
    BAR();
    STAMP(2 + rho);
    v4u f[2][3];
#pragma unroll
    for (int c = 0; c < 3; ++c) f[0][c] = tr2(ap[0] + rho * 12288 + c * 1024, 256);
    SB();
#pragma unroll
    for (int u = 0; u < 4; ++u) {
      const v4u xb = xf[rho & 1][u];
#pragma unroll
      for (int c = 0; c < 3; ++c) {
        if (c == 0) aq = mfma(f[u & 1][0], xb, aq);
        if (c == 1) ak = mfma(f[u & 1][1], xb, ak);
        if (c == 2) av = mfma(f[u & 1][2], xb, av);
        if (u < 3) f[(u + 1) & 1][c] = tr2(ap[u + 1] + rho * 12288 + c * 1024, 256);
        STAT(xb[c]);
        if (rho < 3 && u == 0 && c == 0) WLOAD(rho + 1);
        if (rho < 2 && u == 0 && c == 1) { XLOAD1(rho + 2, 0); XLOAD1(rho + 2, 1); }
        if (rho < 2 && u == 0 && c == 2) { XLOAD1(rho + 2, 2); XLOAD1(rho + 2, 3); }
        if (rho == 2 && u == 0 && c == 1) {
#pragma unroll
          for (int u2 = 0; u2 < 2; ++u2) {
            const int i = 2 * wv + u2;
            const char* src; int dst;
            if (isw1) {
              const int T = b >> 1, s = 16 * (b & 1) + i, e = T - 32;
              const int q = T < 32 ? (T & 15) : (4 * (e & 3) + (e >> 2)), t = T < 32 ? (T >> 4) : 2;
              const int p = ((((s >> 3) - (q >> 2)) & 3) << 3) + (s & 7);
              const int m = p < 8 ? 3 * p + t : 16 + 24 * t + p;
              src = (const char*)w1 + (size_t)s * 44032 + T * 64;
              dst = W1T_OFF + (q * 96 + m) * 1024;
            } else {
              int id = (b - 86) * 12 + i;
              const bool ok = (i < 12) && (id < 1888);
              id = ok ? id : 0;
              const int sb = id >> 4, q = id & 15;
              if (sb < 32) { src = (const char*)wo + sb * 16384 + q * 64; dst = WOT_OFF + (q * 32 + sb) * 1024; }
              else { src = (const char*)w2 + (sb - 32) * 16384 + q * 64; dst = W2T_OFF + (q * 86 + pos_c(sb - 32, q >> 2)) * 1024; }
              if (!ok) dst = DUMMY_OFF;
            }
            rt_dst[u2] = dst;
            glds1((lane >> 2) * (isw1 ? 2752 : 1024) + p4 * 16, src, smb + K1_WV + wv * 6144 + 4096 + u2 * 1024);
          }
        }
        if (rho < 3 && u == 2 && c == 0) { XWAIT(rho + 1, rho == 2 ? 5 : rho == 0 ? 8 : 7); STW(rho + 1, 0); }
        if (rho < 3 && u == 2 && c == 1) { STW(rho + 1, 1); }
        if (rho < 3 && u == 2 && c == 2) { STW(rho + 1, 2); }
        if (rho < 3 && u == 3 && c == 0) { STW(rho + 1, 3); }
        if (rho < 3 && u == 3 && c == 1) { STR(xf[(rho + 1) & 1], 0); STR(xf[(rho + 1) & 1], 1); }
        if (rho < 3 && u == 3 && c == 2) { STR(xf[(rho + 1) & 1], 2); STR(xf[(rho + 1) & 1], 3); }
        SB();
        if (u == 0) {
          if (c == 0) c0 = mfma(ones, f[0][0], c0);
          if (c == 1) c1 = mfma(ones, f[0][1], c1);
          if (c == 2) c2 = mfma(ones, f[0][2], c2);
          if (c == 2) STAT(xb[3]);
          SB();
        }
      }
      if (u > 0) { STAT(xb[3]); SB(); }
    }
    if (rho < 3) asm volatile("s_waitcnt vmcnt(%0)" :: "n"(rho == 2 ? 2 : 4) : "memory");
  }
  STAMP(6);
  unsigned we[8];
  {
    asm volatile("s_waitcnt vmcnt(0)" ::: "memory");
    LAS char* ra = stg + 4096 + (4 * hh + q4) * 64 + p4 * 8;
    const v4u f0 = tr2(ra + bl * 32, 512), f1 = tr2(ra + 1024 + bl * 32, 512);
    __builtin_amdgcn_raw_buffer_store_b128(f0, rs, l16, rt_dst[0], 0);
    __builtin_amdgcn_raw_buffer_store_b128(f1, rs, l16, rt_dst[1], 0);
#pragma unroll
    for (int j = 0; j < 8; ++j) we[j] = wphi[((2 * g + KH) * 16 + 8 * (j >> 2) + 4 * hh + (j & 3)) * 16 + (r & 15)];
    if (isw1) {
      f32x16 ca = mfma(ones, f0, zero16());
      ca = mfma(ones, f1, ca);
      if (hh == 0) ((LAS float*)(sm + K1_C1))[wv * 32 + r] = ca[0];
    }
  }
  *(LAS f32x2*)(sm + K1_MS + (tg * 32 + r) * 32 + KH * 16 + hh * 8) = f32x2{sa + sb, qa + qb};
  if (hh == 0) { LAS float* cs = (LAS float*)(sm + K1_CS) + (r ^ (16 * KH)) * 8 + wv; cs[0] = c0[0]; cs[256] = c1[0]; cs[512] = c2[0]; }
  {
    LAS f32x4* ex = (LAS f32x4*)(stg) + lane;
    ex[0] = f32x4{aq[8], aq[9], aq[10], aq[11]}; ex[64] = f32x4{aq[12], aq[13], aq[14], aq[15]};
    ex[128] = f32x4{ak[8], ak[9], ak[10], ak[11]}; ex[192] = f32x4{ak[12], ak[13], ak[14], ak[15]};
    ex[256] = f32x4{av[8], av[9], av[10], av[11]}; ex[320] = f32x4{av[12], av[13], av[14], av[15]};
  }
  LGKM0();
  BAR();
  STAMP(7);
  float rstd;
  {
    LAS f32x4* ex = (LAS f32x4*)(sm + K1_WV + (wv ^ 4) * 6144) + lane;
    LAS f32x4* sp = (LAS f32x4*)(sm + K1_MS + (tg * 32 + r) * 32);
    LAS f32x4* cp = (LAS f32x4*)(sm + K1_CS + (r ^ (16 * KH)) * 32);
    const f32x4 st = sp[0] + sp[1];
    const f32x4 cb = cp[64] + cp[65];
    const f32x4 e2 = ex[128], e3 = ex[192];
    const float mean = (st[0] + st[2]) * (1.0f / 512.0f);
    const float hz = hh ? 0.f : 1.f;
    const float cs1 = ((cb[0] + cb[1]) + (cb[2] + cb[3])) * hz;
#pragma unroll
    for (int e = 0; e < 4; ++e) { ak[e] += e2[e]; ak[4 + e] += e3[e]; }
    ak = mfma32(cs1, -mean, ak);
    const f32x4 ca = cp[0] + cp[1], cc = cp[128] + cp[129];
    const f32x4 e0 = ex[0], e1 = ex[64], e4 = ex[256], e5 = ex[320];
    const float cs0 = ((ca[0] + ca[1]) + (ca[2] + ca[3])) * hz, cs2 = ((cc[0] + cc[1]) + (cc[2] + cc[3])) * hz;
#pragma unroll
    for (int e = 0; e < 4; ++e) { aq[e] += e0[e]; aq[4 + e] += e1[e]; av[e] += e4[e]; av[4 + e] += e5[e]; }
    const float var = vmax((st[1] + st[3]) * (1.0f / 512.0f) - mean * mean, 0.f);
    rstd = __builtin_amdgcn_rsqf(var + 1e-5f);
    av = mfma32(cs2, -mean, av);
    aq = mfma32(cs0, -mean, aq);
  }
  const unsigned sg = (r >= 16) ? 0x80008000u : 0u;
  const v4u wp = v4u{(we[0] | (we[1] << 16)) ^ sg, (we[2] | (we[3] << 16)) ^ sg, (we[4] | (we[5] << 16)) ^ sg, (we[6] | (we[7] << 16)) ^ sg};
  f32x16 pk = mfma(wp, pack8(ak, 0), zero16());
  {
    LAS char* vi = sm + K1_V + tg * 2048 + r * 64 + KH * 32 + hh * 8;
    *(LAS v2u*)(vi) = v2u{pk2(av[0] * rstd, av[1] * rstd), pk2(av[2] * rstd, av[3] * rstd)};
    *(LAS v2u*)(vi + 16) = v2u{pk2(av[4] * rstd, av[5] * rstd), pk2(av[6] * rstd, av[7] * rstd)};
  }
  dual_softmax_sc(pk, rstd * 1.4426950408889634f);
  {
    LAS char* pi = sm + K1_PK + (tg * 2 + KH) * 2048 + r * 64 + hh * 8;
#pragma unroll
    for (int gq = 0; gq < 4; ++gq) *(LAS v2u*)(pi + gq * 16) = v2u{pk2(pk[4 * gq], pk[4 * gq + 1]), pk2(pk[4 * gq + 2], pk[4 * gq + 3])};
  }
  LGKM0();
  BAR();
  STAMP(8);
  if (tg == 0) {
    f32x16 z = zero16();
    const bool isv = ((r >> 4) == KH);
    LAS char* pa = sm + K1_PK + KH * 2048 + (8 * hh + q4) * 64 + bl * 32 + p4 * 8;
    LAS char* pv = sm + K1_V + (8 * hh + q4) * 64 + bl * 32 + p4 * 8;
#pragma unroll
    for (int ks = 0; ks < 8; ++ks) {
      const v4u a = tr2(pa + (ks >> 1) * 4096 + (ks & 1) * 1024, 256);
      v4u bv = tr2(pv + ks * 1024, 256);
      if (!isv) bv = ones;
      z = mfma(a, bv, z);
    }
    if (isv || ((r & 15) == 0)) {
      GAS f32x4* dst = (GAS f32x4*)(ws + KVP_OFF + (size_t)(tt * 32 + 2 * g + KH) * 2176 + (isv ? (r & 15) : 16) * 128 + hh * 64);
#pragma unroll
      for (int gq = 0; gq < 4; ++gq) dst[gq] = f32x4{z[4 * gq], z[4 * gq + 1], z[4 * gq + 2], z[4 * gq + 3]};
    }
  } else if (tg == 1) {
    if (KH == 0 && isw1 && lane < 32) {
      LAS float* cp = (LAS float*)(sm + K1_C1) + lane;
      float s = 0.f;
#pragma unroll
      for (int k = 0; k < 8; ++k) s += cp[32 * k];
      ((GAS float*)(ws + CS1_OFF))[b * 32 + lane] = s;
    }
    if (KH == 1) { __builtin_amdgcn_raw_buffer_store_b128(v4u{SENT, SENT, SENT, SENT}, rs, l16, KVF_OFF + b * 1024, 0); if (b < 32) __builtin_amdgcn_raw_buffer_store_b128(v4u{SENT, SENT, SENT, SENT}, rs, l16, KVF_OFF + (256 + b) * 1024, 0); }
    if (KH == 1 && lane < 16) ((gu32*)(ws + FLG_OFF))[(b * 16 + lane) * 16] = 0u;
  }
  {
    f32x16 pq = mfma(wp, pack8(aq, 0), zero16());
    dual_softmax_sc(pq, rstd * 1.4426950408889634f);
    const int qo = QF_OFF + ((tt * 4 + tg) * 32 + 2 * g + KH) * 2048;
    __builtin_amdgcn_raw_buffer_store_b128(pack8(pq, 0), rs, l16, qo, 0);
    __builtin_amdgcn_raw_buffer_store_b128(pack8(pq, 1), rs, l16, qo + 1024, 0);
  }
  STAMP(9); STAMP_FLUSH(0);
}

constexpr int K2_OF = 0;
constexpr int K2_HF = 32768;
constexpr int K2_H1 = 65536;
constexpr int K2_ST = 153600;
constexpr int K2_LDS = 154624;
#define KV_GATE 190
constexpr int RDA = 42, RDB = 38;
constexpr int OOB = 0x7FFFFF00;

DI void wait_flags(gu32* f, int j, int lane) {
  const bool need = lane < 4 && lane != j;
#pragma unroll 1
  for (unsigned sp = 0; sp < (1u << 22); ++sp) {
    unsigned v = 1u;
    if (need) v = __hip_atomic_load(f + lane * 16, __ATOMIC_RELAXED, __HIP_MEMORY_SCOPE_AGENT);
    if (__builtin_amdgcn_ballot_w64(v == 0u) == 0ull) break;
    __builtin_amdgcn_s_sleep(1);
  }
  __builtin_amdgcn_fence(__ATOMIC_ACQUIRE, "wavefront");
}
#define VM0() asm volatile("s_waitcnt vmcnt(0)" ::: "memory")
#define SETFLAG(p) do { if (lane == 0) __hip_atomic_store((p), 1u, __ATOMIC_RELAXED, __HIP_MEMORY_SCOPE_AGENT); } while (0)

DI void o_phase(const v4u (&pf)[4][2], __amdgpu_buffer_rsrc_t rs, LAS char* sm, int b, int wv, int lane, unsigned long long tstart) {
  const int r = lane & 31, hh = lane >> 5;
  while (__builtin_amdgcn_s_memrealtime() - tstart < KV_GATE) __builtin_amdgcn_s_sleep(1);
  v4u kv[4][2];
  const int ko = (r < 16) ? r * 64 + hh * 32 : ((r == 16) | (r == 20)) ? 1024 + hh * 32 : OOB;
#pragma unroll 1
  for (unsigned sp = 0; sp < (1u << 22); ++sp) {
    bool bad = false;
#pragma unroll
    for (int hq = 0; hq < 4; ++hq)
#pragma unroll
      for (int s = 0; s < 2; ++s) {
        kv[hq][s] = __builtin_amdgcn_raw_buffer_load_b128(rs, ko + s * 16, KVF_OFF + (b & 7) * KVF_REP + (4 * wv + hq) * 1088, 16);
        bad |= hassent(kv[hq][s]);
      }
    if (__builtin_amdgcn_ballot_w64(bad) == 0ull) break;
    __builtin_amdgcn_s_sleep(4);
  }
#pragma unroll
  for (int hq = 0; hq < 4; ++hq) {
    f32x16 d = mfma(kv[hq][0], pf[hq][0], zero16());
    d = mfma(kv[hq][1], pf[hq][1], d);
    const float inv = __builtin_amdgcn_rcpf(d[8] + 1e-6f);
#pragma unroll
    for (int i = 0; i < 8; ++i) d[i] *= inv;
    *(LAS v4u*)(sm + K2_OF + (4 * wv + hq) * 1024 + lane * 16) = pack8(d, 0);
  }
}

__global__ __launch_bounds__(512, 2) void k2(const unsigned short* __restrict__ x, const unsigned short* __restrict__ b1,
                                             const unsigned short* __restrict__ b2, char* __restrict__ ws, unsigned short* __restrict__ out) {
  extern __shared__ __attribute__((aligned(16))) char sm_[];
  LAS char* sm = (LAS char*)sm_;
  const unsigned smb = (unsigned)(size_t)sm_;
  const int lane = threadIdx.x & 63, wv = __builtin_amdgcn_readfirstlane(threadIdx.x >> 6);
  const int w = wv & 3;
  const int r = lane & 31, hh = lane >> 5;
  const int b = blockIdx.x, c = (b & 7) * 8 + (b >> 5), j = (b >> 3) & 3, q = 4 * j + w;
  const bool three = (4 * w + j) < 11;
  const __amdgpu_buffer_rsrc_t rs = __builtin_amdgcn_make_buffer_rsrc((void*)ws, 0, WS_BYTES, 0x00020000);
  const int l16 = lane * 16;
  const unsigned long long tstart = __builtin_amdgcn_s_memrealtime();
  STAMP_DECL; STAMP(0);
  LAS char* hf = sm + K2_HF + l16;
  LAS char* h1 = sm + K2_H1 + l16;
  if (wv < 4) {
    constexpr int RD = RDA, NTOT = 118;
    const bool red = b < 136;
    f32x4 kp[2] = {{0.f, 0.f, 0.f, 0.f}, {0.f, 0.f, 0.f, 0.f}};
    if (red) {
      const GAS f32x4* pp = (const GAS f32x4*)(ws + KVP_OFF + (size_t)(lane >> 2) * 69632) + (b * 4 + w) * 8 + (lane & 3);
      kp[0] = pp[0]; kp[1] = pp[4];
    }
    v2u xr[4];
#pragma unroll
    for (int g = 0; g < 4; ++g) xr[g] = *(const GAS v2u*)((const char*)x + (size_t)(32 * c + r) * 1024 + (32 * q + 8 * g + 4 * hh) * 2);
    const unsigned bb2 = b2[32 * q + r];
    v4u pf[4][2];
#pragma unroll
    for (int hq = 0; hq < 4; ++hq)
#pragma unroll
      for (int s = 0; s < 2; ++s) pf[hq][s] = __builtin_amdgcn_raw_buffer_load_b128(rs, l16, QF_OFF + ((c * 32 + 4 * wv + hq) * 2 + s) * 1024, 0);
    v4u ring[RD];
    const int sA = WOT_OFF + q * 32768, sC = W2T_OFF + q * 88064 - 32768;
#define RLOAD(i) do { if ((i) < NTOT) ring[(i) % RD] = __builtin_amdgcn_raw_buffer_load_b128(rs, l16, ((i) < 32 ? sA : sC) + (i) * 1024, 0); } while (0)
#define RLOADS(lo, hi) _Pragma("unroll") for (int i_ = (lo); i_ < (hi); ++i_) RLOAD(i_)
    RLOADS(0, 8);
    SB();
    if (red) {
#pragma unroll
      for (int k = 0; k < 2; ++k) {
#pragma unroll
        for (int e = 0; e < 4; ++e) {
          float v = kp[k][e];
          v += __builtin_bit_cast(float, __builtin_amdgcn_update_dpp(0, __builtin_bit_cast(int, v), 0x124, 0xf, 0xf, false));
          v += __builtin_bit_cast(float, __builtin_amdgcn_update_dpp(0, __builtin_bit_cast(int, v), 0x128, 0xf, 0xf, false));
          { auto s2 = __builtin_amdgcn_permlane16_swap(__float_as_uint(v), __float_as_uint(v), false, false); v = __uint_as_float(s2[0]) + __uint_as_float(s2[1]); }
          kp[k][e] = xsum(v);
        }
      }
      const f32x4 kk = (lane & 4) ? kp[1] : kp[0];
      unsigned v0 = pk2(kk[0], kk[1]), v1 = pk2(kk[2], kk[3]);
      v0 = (v0 == SENT) ? 0xFFFEFFFEu : v0; v1 = (v1 == SENT) ? 0xFFFEFFFEu : v1;
      __hip_atomic_store((GAS unsigned long long*)(ws + KVF_OFF + (lane >> 3) * KVF_REP) + (b * 4 + w) * 8 + (lane & 7), (unsigned long long)v0 | ((unsigned long long)v1 << 32), __ATOMIC_RELAXED, __HIP_MEMORY_SCOPE_AGENT);
    }
    RLOADS(8, 16);
    SB();
    STAMP(1);
    o_phase(pf, rs, sm, b, wv, lane, tstart);
    RLOADS(16, 32);
    LGKM0();
    BAR();
    STAMP(2);
    v4u hf0, hf1;
    {
      f32x16 acc;
#pragma unroll
      for (int g = 0; g < 4; ++g) { acc[4 * g] = blo(xr[g][0]); acc[4 * g + 1] = bhi(xr[g][0]); acc[4 * g + 2] = blo(xr[g][1]); acc[4 * g + 3] = bhi(xr[g][1]); }
      LAS char* ob = sm + K2_OF + l16;
      v4u bq[4];
#pragma unroll
      for (int i = 0; i < 3; ++i) bq[i] = *(LAS v4u*)(ob + i * 1024);
#pragma unroll
      for (int i = 0; i < 32; ++i) { if (i + 3 < 32) bq[(i + 3) & 3] = *(LAS v4u*)(ob + (i + 3) * 1024); acc = mfma(ring[i % RD], bq[i & 3], acc); SB(); }
      hf0 = pack8(acc, 0); hf1 = pack8(acc, 1);
    }
    __builtin_amdgcn_raw_buffer_store_b128(hf0, rs, l16, HX_OFF + (c * 32 + 2 * q) * 1024, 16);
    __builtin_amdgcn_raw_buffer_store_b128(hf1, rs, l16, HX_OFF + (c * 32 + 2 * q + 1) * 1024, 16);
    *(LAS v4u*)(hf + (2 * w) * 1024) = hf0;
    *(LAS v4u*)(hf + (2 * w + 1) * 1024) = hf1;
    LGKM0();
    BAR();
    gu32* fl = (gu32*)(ws + FLG_OFF) + ((c * 4 + w) * 4) * 16;
    VM0();
    SETFLAG(fl + j * 16);
    STAMP(3);
    RLOADS(32, 40);
    wait_flags(fl, j, lane);
#pragma unroll
    for (int d = 1; d < 4; ++d)
#pragma unroll
      for (int k = 0; k < 2; ++k) glds1c(l16, ws + HX_OFF + (c * 32 + 2 * (4 * ((j + d) & 3) + w) + k) * 1024, smb + K2_HF + (8 * d + 2 * w + k) * 1024);
    RLOADS(40, 56);
    asm volatile("s_waitcnt vmcnt(16)" ::: "memory");
    BAR();
    STAMP(4);
    {
      const v4u ones = ONES4;
      f32x16 ag = zero16(), as = zero16();
#pragma unroll
      for (int u = 0; u < 8; ++u) {
        const v4u hb = *(LAS v4u*)(hf + (8 * w + u) * 1024);
        ag = mfma(hb, hb, ag);
        as = mfma(ones, hb, as);
      }
      const float dg = diag16(ag, lane);
      if (hh == 0) *(LAS f32x2*)(sm + K2_ST + r * 32 + w * 8) = f32x2{as[0], dg};
    }
    RLOADS(56, 32 + RD);
    f32x16 acc = mfma32(hh ? 0.f : blo(bb2), 1.0f, zero16());
#pragma unroll
    for (int e = 0; e < 4; ++e) { acc[2 * e] += blo(hf0[e]); acc[2 * e + 1] += bhi(hf0[e]); acc[8 + 2 * e] += blo(hf1[e]); acc[9 + 2 * e] += bhi(hf1[e]); }
    LGKM0();
    BAR();
    BAR();
    STAMP(5);
#define CSTEPS(lo, hi) do { v4u bq_[4]; _Pragma("unroll") for (int p_ = (lo); p_ < (lo) + 3; ++p_) bq_[p_ & 3] = *(LAS v4u*)(h1 + p_ * 1024); \
    _Pragma("unroll") for (int p_ = (lo); p_ < (hi); ++p_) { if (p_ + 3 < (hi)) bq_[(p_ + 3) & 3] = *(LAS v4u*)(h1 + (p_ + 3) * 1024); \
      acc = mfma(ring[(32 + p_) % RD], bq_[p_ & 3], acc); RLOAD(32 + p_ + RD); SB(); } } while (0)
#define PUB(T, pos) do { __builtin_amdgcn_raw_buffer_store_b128(*(LAS v4u*)(h1 + (pos) * 1024), rs, l16, H1X_OFF + (c * 86 + 2 * (T)) * 1024, 16); \
    __builtin_amdgcn_raw_buffer_store_b128(*(LAS v4u*)(h1 + ((pos) + 1) * 1024), rs, l16, H1X_OFF + (c * 86 + 2 * (T) + 1) * 1024, 16); } while (0)
#define GATHER(st) _Pragma("unroll") for (int d = 1; d < 4; ++d) _Pragma("unroll") for (int k = 0; k < 2; ++k) \
    glds1c(l16, ws + H1X_OFF + (c * 86 + 2 * (16 * (st) + 4 * ((j + d) & 3) + w) + k) * 1024, smb + K2_H1 + (8 + 24 * (st) + 8 * d + 2 * w + k) * 1024)
    PUB(q, 2 * w);
    VM0();
    SETFLAG(fl + 16384 + j * 16);
    CSTEPS(0, 8);
    BAR();
    STAMP(6);
    PUB(q + 16, 8 + 2 * w);
    wait_flags(fl + 16384, j, lane);
    GATHER(0);
    VM0();
    SETFLAG(fl + 32768 + j * 16);
    CSTEPS(8, 16);
    BAR();
    STAMP(7);
    if (three) PUB(32 + 4 * w + j, 64 + 2 * (4 * w + j));
    VM0();
    SETFLAG(fl + 49152 + j * 16);
    wait_flags(fl + 32768, j, lane);
    GATHER(1);
    CSTEPS(16, 40);
    VM0();
    BAR();
    STAMP(8);
    wait_flags(fl + 49152, j, lane);
#pragma unroll
    for (int d = 1; d < 4; ++d) {
      const int e = 4 * w + ((j + d) & 3);
      if (e < 11) {
        glds1c(l16, ws + H1X_OFF + (c * 86 + 64 + 2 * e) * 1024, smb + K2_H1 + (64 + 2 * e) * 1024);
        glds1c(l16, ws + H1X_OFF + (c * 86 + 65 + 2 * e) * 1024, smb + K2_H1 + (65 + 2 * e) * 1024);
      }
    }
    CSTEPS(40, 64);
    VM0();
    BAR();
    STAMP(9);
    CSTEPS(64, 86);
    STAMP(10);
#pragma unroll
    for (int k = 0; k < 2; ++k) {
      const v4u o = pack8(acc, k);
      auto r0 = __builtin_amdgcn_permlane32_swap(o[0], o[2], false, false);
      auto r1 = __builtin_amdgcn_permlane32_swap(o[1], o[3], false, false);
      *(GAS v4u*)((char*)out + (size_t)(32 * c + r) * 1024 + (32 * q + 16 * k + 8 * hh) * 2) = v4u{r0[0], r1[0], r0[1], r1[1]};
    }
#undef RLOAD
#undef RLOADS
  } else {
    constexpr int RD = RDB, NTOT = 96;
    const int T2 = three ? (32 + 4 * w + j) : q;
    float csa[3], csb[3]; unsigned bb1[3];
#pragma unroll
    for (int t = 0; t < 3; ++t) {
      const int T = t < 2 ? q + 16 * t : T2;
      const GAS float* cp = (const GAS float*)(ws + CS1_OFF) + T * 64 + r;
      csa[t] = cp[0]; csb[t] = cp[32]; bb1[t] = b1[32 * T + r];
    }
    v4u pf[4][2];
#pragma unroll
    for (int hq = 0; hq < 4; ++hq)
#pragma unroll
      for (int s = 0; s < 2; ++s) pf[hq][s] = __builtin_amdgcn_raw_buffer_load_b128(rs, l16, QF_OFF + ((c * 32 + 4 * wv + hq) * 2 + s) * 1024, 0);
    v4u ring[RD];
    const int sB = W1T_OFF + q * 98304;
    const int l3 = three ? l16 : OOB;
#define RLOAD(i) do { if ((i) < NTOT) ring[(i) % RD] = __builtin_amdgcn_raw_buffer_load_b128(rs, ((i) < 24 ? (i) % 3 == 2 : (i) >= 72) ? l3 : l16, sB + (i) * 1024, 0); } while (0)
#define RLOADS(lo, hi) _Pragma("unroll") for (int i_ = (lo); i_ < (hi); ++i_) RLOAD(i_)
    SB();
    STAMP(1);
    o_phase(pf, rs, sm, b, wv, lane, tstart);
    float ab1[3];
#pragma unroll
    for (int t = 0; t < 3; ++t) ab1[t] = hh ? blo(bb1[t]) : csa[t] + csb[t];
    asm volatile("" : "+v"(ab1[0]), "+v"(ab1[1]), "+v"(ab1[2]));
    LGKM0();
    BAR();
    STAMP(2);
    RLOADS(0, RD);
    BAR();
    STAMP(3);
    f32x16 ac[3];
#pragma unroll
    for (int t = 0; t < 3; ++t) ac[t] = zero16();
    v4u hq2[2];
    hq2[0] = *(LAS v4u*)(hf);
#pragma unroll
    for (int p = 0; p < 8; ++p) {
      const v4u hb = hq2[p & 1];
      if (p < 7) hq2[(p + 1) & 1] = *(LAS v4u*)(hf + (p + 1) * 1024);
#pragma unroll
      for (int t = 0; t < 3; ++t) { ac[t] = mfma(ring[(3 * p + t) % RD], hb, ac[t]); RLOAD(3 * p + t + RD); SB(); }
    }
    BAR();
    STAMP(4);
#define BREST(t) do { v4u bq_[4]; _Pragma("unroll") for (int p_ = 8; p_ < 11; ++p_) bq_[p_ & 3] = *(LAS v4u*)(hf + p_ * 1024); \
    _Pragma("unroll") for (int p_ = 8; p_ < 32; ++p_) { if (p_ + 3 < 32) bq_[(p_ + 3) & 3] = *(LAS v4u*)(hf + (p_ + 3) * 1024); \
      ac[t] = mfma(ring[(16 + 24 * (t) + p_) % RD], bq_[p_ & 3], ac[t]); RLOAD(16 + 24 * (t) + p_ + RD); SB(); } } while (0)
#define FINISH(t, pos_) do { ac[t] = mfma32(ab1[t], bop, ac[t]); \
    _Pragma("unroll") for (int e = 0; e < 16; ++e) { const float p_ = vmax(ac[t][e], 0.f); ac[t][e] = (p_ * p_) * r2; } \
    *(LAS v4u*)(h1 + (pos_) * 1024) = pack8(ac[t], 0); *(LAS v4u*)(h1 + ((pos_) + 1) * 1024) = pack8(ac[t], 1); } while (0)
    BREST(0);
    BAR();
    float bop, r2;
    {
      LAS f32x4* sp = (LAS f32x4*)(sm + K2_ST + r * 32);
      const f32x4 st = sp[0] + sp[1];
      const float mean = (st[0] + st[2]) * (1.0f / 512.0f);
      const float var = vmax((st[1] + st[3]) * (1.0f / 512.0f) - mean * mean, 0.f) + 1e-5f;
      const float rstd = __builtin_amdgcn_rsqf(var);
      bop = hh ? var * rstd : -mean;
      r2 = rstd * rstd;
    }
    FINISH(0, 2 * w);
    LGKM0();
    BAR();
    STAMP(5);
    BREST(1);
    FINISH(1, 8 + 2 * w);
    LGKM0();
    BAR();
    STAMP(6);
    if (three) {
      BREST(2);
      FINISH(2, 64 + 2 * (4 * w + j));
    }
    LGKM0();
    BAR();
    STAMP(7);
    BAR();
    BAR();
#undef RLOAD
#undef RLOADS
  }
  STAMP(11); STAMP_FLUSH(1);
}
}

extern "C" void kernel_launch(void* const* d_in, const int* in_sizes, int n_in, void* d_out, int out_size, void* d_ws, size_t ws_size,
                              hipStream_t stream) {
  const unsigned short* x = (const unsigned short*)d_in[0];
  const unsigned short* wqkv = (const unsigned short*)d_in[1];
  const unsigned short* wphi = (const unsigned short*)d_in[2];
  const unsigned short* wo = (const unsigned short*)d_in[3];
  const unsigned short* w1 = (const unsigned short*)d_in[5];
  const unsigned short* b1 = (const unsigned short*)d_in[6];
  const unsigned short* w2 = (const unsigned short*)d_in[7];
  const unsigned short* b2 = (const unsigned short*)d_in[8];
  char* ws = (char*)d_ws;
  (void)hipFuncSetAttribute((const void*)k1, hipFuncAttributeMaxDynamicSharedMemorySize, K1_LDS);
  (void)hipFuncSetAttribute((const void*)k2, hipFuncAttributeMaxDynamicSharedMemorySize, K2_LDS);
  k1<<<256, 512, K1_LDS, stream>>>(x, wqkv, wphi, wo, w1, w2, ws);
  k2<<<256, 512, K2_LDS, stream>>>(x, b1, b2, ws, (unsigned short*)d_out);
}
```

```cpp
#include <hip/hip_runtime.h>
#include <hip/hip_bf16.h>
#include <stdint.h>

#define GAS __attribute__((address_space(1)))
#define LAS __attribute__((address_space(3)))
#define DI __device__ __forceinline__
typedef unsigned v4u __attribute__((ext_vector_type(4)));
typedef unsigned v2u __attribute__((ext_vector_type(2)));
typedef float f32x16 __attribute__((ext_vector_type(16)));
typedef float f32x4 __attribute__((ext_vector_type(4)));
typedef float f32x2 __attribute__((ext_vector_type(2)));
typedef __bf16 bf16x8 __attribute__((ext_vector_type(8)));
typedef __bf16 bf16x2 __attribute__((ext_vector_type(2)));
typedef short s16x4 __attribute__((ext_vector_type(4)));
typedef GAS unsigned gu32;

#define STAMP_DECL
#define STAMP(i)
#define STAMP_FLUSH(k)

namespace {
constexpr int WOT_OFF = 0;
constexpr int W1T_OFF = 512 << 10;
constexpr int W2T_OFF = 2048 << 10;
constexpr int QF_OFF = 16 << 20;
constexpr int KVP_OFF = 6 << 20;
constexpr int KVF_OFF = (7 << 20) + (128 << 10);
constexpr int KVF_REP = 36864;
constexpr int CS1_OFF = (7 << 20) + (640 << 10);
constexpr int WPF_OFF = (7 << 20) + (704 << 10);
constexpr int DUMMY_OFF = (7 << 20) + (768 << 10);
constexpr int HX_OFF = 8 << 20;
constexpr int H1X_OFF = 10 << 20;
constexpr int FLG_OFF = (15 << 20) + (512 << 10);
constexpr int WS_BYTES = 20 << 20;
constexpr unsigned SENT = 0xFFFFFFFFu;

DI f32x16 mfma(v4u a, v4u b, f32x16 c) {
  return __builtin_amdgcn_mfma_f32_32x32x16_bf16(__builtin_bit_cast(bf16x8, a), __builtin_bit_cast(bf16x8, b), c, 0, 0, 0);
}
DI f32x16 mfma32(float a, float b, f32x16 c) { return __builtin_amdgcn_mfma_f32_32x32x2f32(a, b, c, 0, 0, 0); }
DI unsigned pk2(float a, float b) { bf16x2 v; v[0] = (__bf16)a; v[1] = (__bf16)b; return __builtin_bit_cast(unsigned, v); }
DI float blo(unsigned v) { return __uint_as_float(v << 16); }
DI float bhi(unsigned v) { return __uint_as_float(v & 0xffff0000u); }
DI v4u pack8(const f32x16& x, int s) {
  return v4u{pk2(x[8 * s + 0], x[8 * s + 1]), pk2(x[8 * s + 2], x[8 * s + 3]), pk2(x[8 * s + 4], x[8 * s + 5]), pk2(x[8 * s + 6], x[8 * s + 7])};
}
DI v4u nosent(v4u v) {
#pragma unroll
  for (int i = 0; i < 4; ++i) v[i] = (v[i] == SENT) ? 0xFFFEFFFEu : v[i];
  return v;
}
DI bool hassent(v4u v) { return (v[0] == SENT) | (v[1] == SENT) | (v[2] == SENT) | (v[3] == SENT); }
DI float xsum(float v) { auto r = __builtin_amdgcn_permlane32_swap(__float_as_uint(v), __float_as_uint(v), false, false); return __uint_as_float(r[0]) + __uint_as_float(r[1]); }
DI float vmax(float a, float b) { return __builtin_elementwise_maximum(a, b); }
DI v2u trread(LAS char* p) { return __builtin_bit_cast(v2u, __builtin_amdgcn_ds_read_tr16_b64_v4i16((LAS s16x4*)p)); }
DI v4u tr2(LAS char* a, int d) { v2u l = trread(a), h = trread(a + d); return v4u{l[0], l[1], h[0], h[1]}; }
DI f32x16 zero16() { f32x16 z; for (int i = 0; i < 16; ++i) z[i] = 0.f; return z; }
DI float diag16(const f32x16& d, int lane) {
  const int r = lane & 31, hh = lane >> 5;
  const int istar = (r & 3) + 4 * (r >> 3);
  float v = 0.f;
#pragma unroll
  for (int i = 0; i < 16; ++i) v = (i == istar) ? d[i] : v;
  v = (hh == ((r >> 2) & 1)) ? v : 0.f;
  return xsum(v);
}
DI void dual_softmax_sc(f32x16& p, float sc) {
#pragma unroll
  for (int hf = 0; hf < 2; ++hf) {
    float m = vmax(vmax(vmax(p[8 * hf], p[8 * hf + 1]), vmax(p[8 * hf + 2], p[8 * hf + 3])), vmax(vmax(p[8 * hf + 4], p[8 * hf + 5]), vmax(p[8 * hf + 6], p[8 * hf + 7])));
    { auto rr = __builtin_amdgcn_permlane32_swap(__float_as_uint(m), __float_as_uint(m), false, false); m = vmax(__uint_as_float(rr[0]), __uint_as_float(rr[1])); }
    m = -(m * sc);
    float s = 0.f;
#pragma unroll
    for (int i = 0; i < 8; ++i) { float e = __builtin_amdgcn_exp2f(__builtin_fmaf(p[8 * hf + i], sc, m)); p[8 * hf + i] = e; s += e; }
    s = xsum(s);
    const float inv = __builtin_amdgcn_rcpf(s);
#pragma unroll
    for (int i = 0; i < 8; ++i) p[8 * hf + i] *= inv;
  }
}
DI void glds3(unsigned voff, const void* sbase, unsigned lds_dst) {
  unsigned keep;
  asm volatile("s_mov_b32 %0, m0\n\ts_mov_b32 m0, %3\n\ts_nop 0\n\tglobal_load_lds_dwordx4 %1, %2\n\tglobal_load_lds_dwordx4 %1, %2 offset:1024\n\t"
               "global_load_lds_dwordx4 %1, %2 offset:2048\n\ts_mov_b32 m0, %0" : "=&s"(keep) : "v"(voff), "s"(sbase), "s"(lds_dst) : "memory");
}
DI void glds1(unsigned voff, const void* sbase, unsigned lds_dst) {
  unsigned keep;
  asm volatile("s_mov_b32 %0, m0\n\ts_mov_b32 m0, %3\n\ts_nop 0\n\tglobal_load_lds_dwordx4 %1, %2\n\ts_mov_b32 m0, %0" : "=&s"(keep) : "v"(voff), "s"(sbase), "s"(lds_dst) : "memory");
}
DI void glds1c(unsigned voff, const void* sbase, unsigned lds_dst) {
  unsigned keep;
  asm volatile("s_mov_b32 %0, m0\n\ts_mov_b32 m0, %3\n\ts_nop 0\n\tglobal_load_lds_dwordx4 %1, %2 sc1\n\ts_mov_b32 m0, %0" : "=&s"(keep) : "v"(voff), "s"(sbase), "s"(lds_dst) : "memory");
}
#define SB() __builtin_amdgcn_sched_barrier(0)
#define LGKM0() asm volatile("s_waitcnt lgkmcnt(0)" ::: "memory")
#define BAR() __builtin_amdgcn_s_barrier()
#define ONES4 v4u{0x3f803f80u, 0x3f803f80u, 0x3f803f80u, 0x3f803f80u}
DI int pos_c(int s, int j) {
  const int T = s >> 1;
  if (T >= 32) return s;
  const int st = T >> 4, d = (((T >> 2) & 3) - j) & 3, lo = 2 * (T & 3) + (s & 1);
  return d == 0 ? 8 * st + lo : 8 + 24 * st + 8 * d + lo;
}

constexpr int K1_WL = 0;
constexpr int K1_PK = 0;
constexpr int K1_V = 16384;
constexpr int K1_WV = 98304;
constexpr int K1_CS = 147456;
constexpr int K1_MS = 150528;
constexpr int K1_C1 = 154624;
constexpr int K1_LDS = 156672;
constexpr unsigned CODE_SPAN = 25000;

__global__ __launch_bounds__(512, 2) void k1(const unsigned short* __restrict__ x, const unsigned short* __restrict__ wqkv,
                                             const unsigned short* __restrict__ wphi, const unsigned short* __restrict__ wo,
                                             const unsigned short* __restrict__ w1, const unsigned short* __restrict__ w2, char* __restrict__ ws) {
  extern __shared__ __attribute__((aligned(16))) char sm_[];
  LAS char* sm = (LAS char*)sm_;
  const unsigned smb = (unsigned)(size_t)sm_;
  const int lane = threadIdx.x & 63, wv = __builtin_amdgcn_readfirstlane(threadIdx.x >> 6);
  const int tg = wv & 3, KH = wv >> 2;
  const int r = lane & 31, hh = lane >> 5;
  const int b = blockIdx.x, xcd = b & 7, bi = b >> 3;
  const int tt = 4 * (xcd >> 1) + (bi & 3), g = 8 * (xcd & 1) + (bi >> 2);
  const int row8 = lane >> 3, part = lane & 7, p4 = lane & 3;
  const __amdgpu_buffer_rsrc_t rs = __builtin_amdgcn_make_buffer_rsrc((void*)ws, 0, WS_BYTES, 0x00020000);
  const int l16 = lane * 16;
  STAMP_DECL; STAMP(0);
  v4u xr[16];
  const unsigned xvo = row8 * 1024 + part * 16;
  const char* xsb = (const char*)x + (size_t)(tt * 128 + tg * 32) * 1024 + KH * 512;
  const unsigned wvo = (lane >> 2) * 3072 + p4 * 16;
  const char* wsb = (const char*)wqkv + (size_t)(256 * KH + 16 * tg) * 3072 + g * 64;
  const unsigned wld = smb + K1_WL + ((16 * KH + tg) * 3) * 1024;
#define XLOAD1(R, rg) asm volatile("global_load_dwordx4 %0, %1, %2 offset:%3" : "=v"(xr[4 * (R) + (rg)]) : "v"(xvo), "s"(xsb + (rg) * 8192), "n"((R) * 128) : "memory")
#define XLOAD(R) do { XLOAD1(R, 0); XLOAD1(R, 1); XLOAD1(R, 2); XLOAD1(R, 3); } while (0)
#define WLOAD(R) glds3(wvo, wsb + (size_t)(R) * 64 * 3072, wld + (R) * 12288)
  XLOAD(0); WLOAD(0); XLOAD(1);
  {
    const unsigned long long pc = __builtin_amdgcn_s_getpc() & ~255ull;
    unsigned off = (unsigned)threadIdx.x * 64u;
    off = off < CODE_SPAN ? off : CODE_SPAN;
    unsigned keep;
    asm volatile("s_mov_b32 %0, m0\n\ts_mov_b32 m0, %3\n\ts_nop 0\n\tglobal_load_lds_dword %1, %2\n\ts_mov_b32 m0, %0" : "=&s"(keep) : "v"(off), "s"(pc), "s"(smb + K1_C1 + wv * 256) : "memory");
  }
  STAMP(1);
  const bool isw1 = b < 86;
  int rt_dst[2];
  f32x16 aq = zero16(), ak = zero16(), av = zero16(), c0 = zero16(), c1 = zero16(), c2 = zero16();
  const v4u ones = ONES4;
  float sa = 0.f, sb = 0.f, qa = 0.f, qb = 0.f;
  const int bl = (lane >> 4) & 1, q4 = (lane & 15) >> 2;
  LAS char* ap0 = sm + K1_WL + (16 * KH) * 3072 + (8 * hh + q4) * 64 + (bl ^ KH) * 32 + p4 * 8;
  LAS char* ap[4] = {ap0 + tg * 3072, ap0 + (tg ^ 1) * 3072, ap0 + (tg ^ 2) * 3072, ap0 + (tg ^ 3) * 3072};
  LAS char* stg = sm + K1_WV + wv * 6144;
  LAS char* stw0 = stg + row8 * 128 + ((part ^ (row8 >> 1)) << 4);
  LAS char* stw1 = stg + row8 * 128 + ((part ^ (row8 >> 1) ^ 4) << 4);
  const unsigned srd = (r * 128 + ((hh ^ ((r >> 1) & 7)) << 4)) ^ (tg << 5);
#define XWAIT(R, N) asm volatile("s_waitcnt vmcnt(%4)" : "+v"(xr[4 * (R)]), "+v"(xr[4 * (R) + 1]), "+v"(xr[4 * (R) + 2]), "+v"(xr[4 * (R) + 3]) : "n"(N) : "memory")
#define STW(R, k) *(LAS v4u*)(((k) & 1 ? stw1 : stw0) + (k) * 1024) = xr[4 * (R) + (k)]
#define STR(XF, k) XF[k] = *(LAS v4u*)(stg + (srd ^ ((k) << 5)))
#define STAT(v) do { const float lo_ = blo(v), hi_ = bhi(v); sa += lo_; sb += hi_; qa = __builtin_fmaf(lo_, lo_, qa); qb = __builtin_fmaf(hi_, hi_, qb); \
    asm volatile("" : "+v"(sa), "+v"(sb), "+v"(qa), "+v"(qb)); } while (0)
  v4u xf[2][4];
  XWAIT(0, 8);
  STW(0, 0); STW(0, 1); STW(0, 2); STW(0, 3); STR(xf[0], 0); STR(xf[0], 1); STR(xf[0], 2); STR(xf[0], 3);
  asm volatile("s_waitcnt vmcnt(5)" ::: "memory");
#pragma unroll
  for (int rho = 0; rho < 4; ++rho) {
    BAR();
    STAMP(2 + rho);
    v4u f[2][3];
#pragma unroll
    for (int c = 0; c < 3; ++c) f[0][c] = tr2(ap[0] + rho * 12288 + c * 1024, 256);
    SB();
#pragma unroll
    for (int u = 0; u < 4; ++u) {
      const v4u xb = xf[rho & 1][u];
#pragma unroll
      for (int c = 0; c < 3; ++c) {
        if (c == 0) aq = mfma(f[u & 1][0], xb, aq);
        if (c == 1) ak = mfma(f[u & 1][1], xb, ak);
        if (c == 2) av = mfma(f[u & 1][2], xb, av);
        if (u < 3) f[(u + 1) & 1][c] = tr2(ap[u + 1] + rho * 12288 + c * 1024, 256);
        STAT(xb[c]);
        if (rho == 0 && u == 0 && c == 0) WLOAD(1);
        if (rho == 0 && u == 1 && c == 1) WLOAD(2);
        if (rho == 1 && u == 1 && c == 0) WLOAD(3);
        if (rho < 2 && u == 0 && c == 1) { XLOAD1(rho + 2, 0); XLOAD1(rho + 2, 1); }
        if (rho < 2 && u == 0 && c == 2) { XLOAD1(rho + 2, 2); XLOAD1(rho + 2, 3); }
        if (rho == 1 && u == 1 && c == 2) {
#pragma unroll
          for (int u2 = 0; u2 < 2; ++u2) {
            const int i = 2 * wv + u2;
            const char* src; int dst;
            if (isw1) {
              const int T = b >> 1, s = 16 * (b & 1) + i, e = T - 32;
              const int q = T < 32 ? (T & 15) : (4 * (e & 3) + (e >> 2)), t = T < 32 ? (T >> 4) : 2;
              const int p = ((((s >> 3) - (q >> 2)) & 3) << 3) + (s & 7);
              const int m = p < 8 ? 3 * p + t : 16 + 24 * t + p;
              src = (const char*)w1 + (size_t)s * 44032 + T * 64;
              dst = W1T_OFF + (q * 96 + m) * 1024;
            } else {
              int id = (b - 86) * 12 + i;
              const bool ok = (i < 12) && (id < 1888);
              id = ok ? id : 0;
              const int sb = id >> 4, q = id & 15;
              if (sb < 32) { src = (const char*)wo + sb * 16384 + q * 64; dst = WOT_OFF + (q * 32 + sb) * 1024; }
              else { src = (const char*)w2 + (sb - 32) * 16384 + q * 64; dst = W2T_OFF + (q * 86 + pos_c(sb - 32, q >> 2)) * 1024; }
              if (!ok) dst = DUMMY_OFF;
            }
            rt_dst[u2] = dst;
            glds1((lane >> 2) * (isw1 ? 2752 : 1024) + p4 * 16, src, smb + K1_WV + wv * 6144 + 4096 + u2 * 1024);
          }
        }
        if (rho < 3 && u == 2 && c == 0) { XWAIT(rho + 1, rho == 2 ? 5 : rho == 0 ? 11 : 12); STW(rho + 1, 0); }
        if (rho < 3 && u == 2 && c == 1) { STW(rho + 1, 1); }
        if (rho < 3 && u == 2 && c == 2) { STW(rho + 1, 2); }
        if (rho < 3 && u == 3 && c == 0) { STW(rho + 1, 3); }
        if (rho < 3 && u == 3 && c == 1) { STR(xf[(rho + 1) & 1], 0); STR(xf[(rho + 1) & 1], 1); }
        if (rho < 3 && u == 3 && c == 2) { STR(xf[(rho + 1) & 1], 2); STR(xf[(rho + 1) & 1], 3); }
        SB();
        if (u == 0) {
          if (c == 0) c0 = mfma(ones, f[0][0], c0);
          if (c == 1) c1 = mfma(ones, f[0][1], c1);
          if (c == 2) c2 = mfma(ones, f[0][2], c2);
          if (c == 2) STAT(xb[3]);
          SB();
        }
      }
      if (u > 0) { STAT(xb[3]); SB(); }
    }
    if (rho < 3) asm volatile("s_waitcnt vmcnt(%0)" :: "n"(rho == 2 ? 2 : rho == 0 ? 7 : 9) : "memory");
  }
  STAMP(6);
  unsigned we[8];
  {
    asm volatile("s_waitcnt vmcnt(0)" ::: "memory");
    LAS char* ra = stg + 4096 + (4 * hh + q4) * 64 + p4 * 8;
    const v4u f0 = tr2(ra + bl * 32, 512), f1 = tr2(ra + 1024 + bl * 32, 512);
    __builtin_amdgcn_raw_buffer_store_b128(f0, rs, l16, rt_dst[0], 0);
    __builtin_amdgcn_raw_buffer_store_b128(f1, rs, l16, rt_dst[1], 0);
#pragma unroll
    for (int j = 0; j < 8; ++j) we[j] = wphi[((2 * g + KH) * 16 + 8 * (j >> 2) + 4 * hh + (j & 3)) * 16 + (r & 15)];
    if (isw1) {
      f32x16 ca = mfma(ones, f0, zero16());
      ca = mfma(ones, f1, ca);
      if (hh == 0) ((LAS float*)(sm + K1_C1))[wv * 32 + r] = ca[0];
    }
  }
  *(LAS f32x2*)(sm + K1_MS + (tg * 32 + r) * 32 + KH * 16 + hh * 8) = f32x2{sa + sb, qa + qb};
  if (hh == 0) { LAS float* cs = (LAS float*)(sm + K1_CS) + (r ^ (16 * KH)) * 8 + wv; cs[0] = c0[0]; cs[256] = c1[0]; cs[512] = c2[0]; }
  {
    LAS f32x4* ex = (LAS f32x4*)(stg) + lane;
    ex[0] = f32x4{aq[8], aq[9], aq[10], aq[11]}; ex[64] = f32x4{aq[12], aq[13], aq[14], aq[15]};
    ex[128] = f32x4{ak[8], ak[9], ak[10], ak[11]}; ex[192] = f32x4{ak[12], ak[13], ak[14], ak[15]};
    ex[256] = f32x4{av[8], av[9], av[10], av[11]}; ex[320] = f32x4{av[12], av[13], av[14], av[15]};
  }
  LGKM0();
  BAR();
  STAMP(7);
  float rstd;
  {
    LAS f32x4* ex = (LAS f32x4*)(sm + K1_WV + (wv ^ 4) * 6144) + lane;
    LAS f32x4* sp = (LAS f32x4*)(sm + K1_MS + (tg * 32 + r) * 32);
    LAS f32x4* cp = (LAS f32x4*)(sm + K1_CS + (r ^ (16 * KH)) * 32);
    const f32x4 st = sp[0] + sp[1];
    const f32x4 cb = cp[64] + cp[65];
    const f32x4 e2 = ex[128], e3 = ex[192];
    const float mean = (st[0] + st[2]) * (1.0f / 512.0f);
    const float hz = hh ? 0.f : 1.f;
    const float cs1 = ((cb[0] + cb[1]) + (cb[2] + cb[3])) * hz;
#pragma unroll
    for (int e = 0; e < 4; ++e) { ak[e] += e2[e]; ak[4 + e] += e3[e]; }
    ak = mfma32(cs1, -mean, ak);
    const f32x4 ca = cp[0] + cp[1], cc = cp[128] + cp[129];
    const f32x4 e0 = ex[0], e1 = ex[64], e4 = ex[256], e5 = ex[320];
    const float cs0 = ((ca[0] + ca[1]) + (ca[2] + ca[3])) * hz, cs2 = ((cc[0] + cc[1]) + (cc[2] + cc[3])) * hz;
#pragma unroll
    for (int e = 0; e < 4; ++e) { aq[e] += e0[e]; aq[4 + e] += e1[e]; av[e] += e4[e]; av[4 + e] += e5[e]; }
    const float var = vmax((st[1] + st[3]) * (1.0f / 512.0f) - mean * mean, 0.f);
    rstd = __builtin_amdgcn_rsqf(var + 1e-5f);
    av = mfma32(cs2, -mean, av);
    aq = mfma32(cs0, -mean, aq);
  }
  const unsigned sg = (r >= 16) ? 0x80008000u : 0u;
  const v4u wp = v4u{(we[0] | (we[1] << 16)) ^ sg, (we[2] | (we[3] << 16)) ^ sg, (we[4] | (we[5] << 16)) ^ sg, (we[6] | (we[7] << 16)) ^ sg};
  f32x16 pk = mfma(wp, pack8(ak, 0), zero16());
  {
    LAS char* vi = sm + K1_V + tg * 2048 + r * 64 + KH * 32 + hh * 8;
    *(LAS v2u*)(vi) = v2u{pk2(av[0] * rstd, av[1] * rstd), pk2(av[2] * rstd, av[3] * rstd)};
    *(LAS v2u*)(vi + 16) = v2u{pk2(av[4] * rstd, av[5] * rstd), pk2(av[6] * rstd, av[7] * rstd)};
  }
  dual_softmax_sc(pk, rstd * 1.4426950408889634f);
  {
    LAS char* pi = sm + K1_PK + (tg * 2 + KH) * 2048 + r * 64 + hh * 8;
#pragma unroll
    for (int gq = 0; gq < 4; ++gq) *(LAS v2u*)(pi + gq * 16) = v2u{pk2(pk[4 * gq], pk[4 * gq + 1]), pk2(pk[4 * gq + 2], pk[4 * gq + 3])};
  }
  LGKM0();
  BAR();
  STAMP(8);
  if (tg == 0) {
    f32x16 z = zero16();
    const bool isv = ((r >> 4) == KH);
    LAS char* pa = sm + K1_PK + KH * 2048 + (8 * hh + q4) * 64 + bl * 32 + p4 * 8;
    LAS char* pv = sm + K1_V + (8 * hh + q4) * 64 + bl * 32 + p4 * 8;
#pragma unroll
    for (int ks = 0; ks < 8; ++ks) {
      const v4u a = tr2(pa + (ks >> 1) * 4096 + (ks & 1) * 1024, 256);
      v4u bv = tr2(pv + ks * 1024, 256);
      if (!isv) bv = ones;
      z = mfma(a, bv, z);
    }
    if (isv || ((r & 15) == 0)) {
      GAS f32x4* dst = (GAS f32x4*)(ws + KVP_OFF + (size_t)(tt * 32 + 2 * g + KH) * 2176 + (isv ? (r & 15) : 16) * 128 + hh * 64);
#pragma unroll
      for (int gq = 0; gq < 4; ++gq) dst[gq] = f32x4{z[4 * gq], z[4 * gq + 1], z[4 * gq + 2], z[4 * gq + 3]};
    }
  } else if (tg == 1) {
    if (KH == 0 && isw1 && lane < 32) {
      LAS float* cp = (LAS float*)(sm + K1_C1) + lane;
      float s = 0.f;
#pragma unroll
      for (int k = 0; k < 8; ++k) s += cp[32 * k];
      ((GAS float*)(ws + CS1_OFF))[b * 32 + lane] = s;
    }
    if (KH == 1) { __builtin_amdgcn_raw_buffer_store_b128(v4u{SENT, SENT, SENT, SENT}, rs, l16, KVF_OFF + b * 1024, 0); if (b < 32) __builtin_amdgcn_raw_buffer_store_b128(v4u{SENT, SENT, SENT, SENT}, rs, l16, KVF_OFF + (256 + b) * 1024, 0); }
    if (KH == 1 && lane < 16) ((gu32*)(ws + FLG_OFF))[(b * 16 + lane) * 16] = 0u;
  }
  {
    f32x16 pq = mfma(wp, pack8(aq, 0), zero16());
    dual_softmax_sc(pq, rstd * 1.4426950408889634f);
    const int qo = QF_OFF + ((tt * 4 + tg) * 32 + 2 * g + KH) * 2048;
    __builtin_amdgcn_raw_buffer_store_b128(pack8(pq, 0), rs, l16, qo, 0);
    __builtin_amdgcn_raw_buffer_store_b128(pack8(pq, 1), rs, l16, qo + 1024, 0);
  }
  STAMP(9); STAMP_FLUSH(0);
}

constexpr int K2_OF = 0;
constexpr int K2_HF = 32768;
constexpr int K2_H1 = 65536;
constexpr int K2_ST = 153600;
constexpr int K2_LDS = 154624;
#define KV_GATE 235
constexpr int RDA = 42, RDB = 38;
constexpr int OOB = 0x7FFFFF00;

DI void wait_flags(gu32* f, int j, int lane) {
  const bool need = lane < 4 && lane != j;
#pragma unroll 1
  for (unsigned sp = 0; sp < (1u << 22); ++sp) {
    unsigned v = 1u;
    if (need) v = __hip_atomic_load(f + lane * 16, __ATOMIC_RELAXED, __HIP_MEMORY_SCOPE_AGENT);
    if (__builtin_amdgcn_ballot_w64(v == 0u) == 0ull) break;
    __builtin_amdgcn_s_sleep(1);
  }
  __builtin_amdgcn_fence(__ATOMIC_ACQUIRE, "wavefront");
}
#define VM0() asm volatile("s_waitcnt vmcnt(0)" ::: "memory")
#define SETFLAG(p) do { if (lane == 0) __hip_atomic_store((p), 1u, __ATOMIC_RELAXED, __HIP_MEMORY_SCOPE_AGENT); } while (0)

DI void o_phase(const v4u (&pf)[4][2], __amdgpu_buffer_rsrc_t rs, LAS char* sm, int b, int wv, int lane, unsigned long long tstart) {
  const int r = lane & 31, hh = lane >> 5;
  while (__builtin_amdgcn_s_memrealtime() - tstart < KV_GATE) __builtin_amdgcn_s_sleep(1);
  v4u kv[4][2];
  const int ko = (r < 16) ? r * 64 + hh * 32 : ((r == 16) | (r == 20)) ? 1024 + hh * 32 : OOB;
#pragma unroll 1
  for (unsigned sp = 0; sp < (1u << 22); ++sp) {
    bool bad = false;
#pragma unroll
    for (int hq = 0; hq < 4; ++hq)
#pragma unroll
      for (int s = 0; s < 2; ++s) {
        kv[hq][s] = __builtin_amdgcn_raw_buffer_load_b128(rs, ko + s * 16, KVF_OFF + (b & 7) * KVF_REP + (4 * wv + hq) * 1088, 16);
        bad |= hassent(kv[hq][s]);
      }
    if (__builtin_amdgcn_ballot_w64(bad) == 0ull) break;
    __builtin_amdgcn_s_sleep(4);
  }
#pragma unroll
  for (int hq = 0; hq < 4; ++hq) {
    f32x16 d = mfma(kv[hq][0], pf[hq][0], zero16());
    d = mfma(kv[hq][1], pf[hq][1], d);
    const float inv = __builtin_amdgcn_rcpf(d[8] + 1e-6f);
#pragma unroll
    for (int i = 0; i < 8; ++i) d[i] *= inv;
    *(LAS v4u*)(sm + K2_OF + (4 * wv + hq) * 1024 + lane * 16) = pack8(d, 0);
  }
}

__global__ __launch_bounds__(512, 2) void k2(const unsigned short* __restrict__ x, const unsigned short* __restrict__ b1,
                                             const unsigned short* __restrict__ b2, char* __restrict__ ws, unsigned short* __restrict__ out) {
  extern __shared__ __attribute__((aligned(16))) char sm_[];
  LAS char* sm = (LAS char*)sm_;
  const unsigned smb = (unsigned)(size_t)sm_;
  const int lane = threadIdx.x & 63, wv = __builtin_amdgcn_readfirstlane(threadIdx.x >> 6);
  const int w = wv & 3;
  const int r = lane & 31, hh = lane >> 5;
  const int b = blockIdx.x, c = (b & 7) * 8 + (b >> 5), j = (b >> 3) & 3, q = 4 * j + w;
  const bool three = (4 * w + j) < 11;
  const __amdgpu_buffer_rsrc_t rs = __builtin_amdgcn_make_buffer_rsrc((void*)ws, 0, WS_BYTES, 0x00020000);
  const int l16 = lane * 16;
  const unsigned long long tstart = __builtin_amdgcn_s_memrealtime();
  STAMP_DECL; STAMP(0);
  LAS char* hf = sm + K2_HF + l16;
  LAS char* h1 = sm + K2_H1 + l16;
  if (wv < 4) {
    constexpr int RD = RDA, NTOT = 118;
    const bool red = b < 136;
    f32x4 kp[2] = {{0.f, 0.f, 0.f, 0.f}, {0.f, 0.f, 0.f, 0.f}};
    if (red) {
      const GAS f32x4* pp = (const GAS f32x4*)(ws + KVP_OFF + (size_t)(lane >> 2) * 69632) + (b * 4 + w) * 8 + (lane & 3);
      kp[0] = pp[0]; kp[1] = pp[4];
    }
    v2u xr[4];
#pragma unroll
    for (int g = 0; g < 4; ++g) xr[g] = *(const GAS v2u*)((const char*)x + (size_t)(32 * c + r) * 1024 + (32 * q + 8 * g + 4 * hh) * 2);
    const unsigned bb2 = b2[32 * q + r];
    v4u pf[4][2];
#pragma unroll
    for (int hq = 0; hq < 4; ++hq)
#pragma unroll
      for (int s = 0; s < 2; ++s) pf[hq][s] = __builtin_amdgcn_raw_buffer_load_b128(rs, l16, QF_OFF + ((c * 32 + 4 * wv + hq) * 2 + s) * 1024, 0);
    v4u ring[RD];
    const int sA = WOT_OFF + q * 32768, sC = W2T_OFF + q * 88064 - 32768;
#define RLOAD(i) do { if ((i) < NTOT) ring[(i) % RD] = __builtin_amdgcn_raw_buffer_load_b128(rs, l16, ((i) < 32 ? sA : sC) + (i) * 1024, 0); } while (0)
#define RLOADS(lo, hi) _Pragma("unroll") for (int i_ = (lo); i_ < (hi); ++i_) RLOAD(i_)
    RLOADS(0, 8);
    SB();
    if (red) {
#pragma unroll
      for (int k = 0; k < 2; ++k) {
#pragma unroll
        for (int e = 0; e < 4; ++e) {
          float v = kp[k][e];
          v += __builtin_bit_cast(float, __builtin_amdgcn_update_dpp(0, __builtin_bit_cast(int, v), 0x124, 0xf, 0xf, false));
          v += __builtin_bit_cast(float, __builtin_amdgcn_update_dpp(0, __builtin_bit_cast(int, v), 0x128, 0xf, 0xf, false));
          { auto s2 = __builtin_amdgcn_permlane16_swap(__float_as_uint(v), __float_as_uint(v), false, false); v = __uint_as_float(s2[0]) + __uint_as_float(s2[1]); }
          kp[k][e] = xsum(v);
        }
      }
      const f32x4 kk = (lane & 4) ? kp[1] : kp[0];
      unsigned v0 = pk2(kk[0], kk[1]), v1 = pk2(kk[2], kk[3]);
      v0 = (v0 == SENT) ? 0xFFFEFFFEu : v0; v1 = (v1 == SENT) ? 0xFFFEFFFEu : v1;
      __hip_atomic_store((GAS unsigned long long*)(ws + KVF_OFF + (lane >> 3) * KVF_REP) + (b * 4 + w) * 8 + (lane & 7), (unsigned long long)v0 | ((unsigned long long)v1 << 32), __ATOMIC_RELAXED, __HIP_MEMORY_SCOPE_AGENT);
    }
    RLOADS(8, 16);
    SB();
    STAMP(1);
    o_phase(pf, rs, sm, b, wv, lane, tstart);
    RLOADS(16, 32);
    LGKM0();
    BAR();
    STAMP(2);
    v4u hf0, hf1;
    {
      f32x16 acc;
#pragma unroll
      for (int g = 0; g < 4; ++g) { acc[4 * g] = blo(xr[g][0]); acc[4 * g + 1] = bhi(xr[g][0]); acc[4 * g + 2] = blo(xr[g][1]); acc[4 * g + 3] = bhi(xr[g][1]); }
      LAS char* ob = sm + K2_OF + l16;
      v4u bq[4];
#pragma unroll
      for (int i = 0; i < 3; ++i) bq[i] = *(LAS v4u*)(ob + i * 1024);
#pragma unroll
      for (int i = 0; i < 32; ++i) { if (i + 3 < 32) bq[(i + 3) & 3] = *(LAS v4u*)(ob + (i + 3) * 1024); acc = mfma(ring[i % RD], bq[i & 3], acc); SB(); }
      hf0 = pack8(acc, 0); hf1 = pack8(acc, 1);
    }
    __builtin_amdgcn_raw_buffer_store_b128(hf0, rs, l16, HX_OFF + (c * 32 + 2 * q) * 1024, 16);
    __builtin_amdgcn_raw_buffer_store_b128(hf1, rs, l16, HX_OFF + (c * 32 + 2 * q + 1) * 1024, 16);
    *(LAS v4u*)(hf + (2 * w) * 1024) = hf0;
    *(LAS v4u*)(hf + (2 * w + 1) * 1024) = hf1;
    gu32* fl = (gu32*)(ws + FLG_OFF) + ((c * 4 + w) * 4) * 16;
    VM0();
    SETFLAG(fl + j * 16);
    LGKM0();
    BAR();
    STAMP(3);
    RLOADS(32, 40);
    wait_flags(fl, j, lane);
#pragma unroll
    for (int d = 1; d < 4; ++d)
#pragma unroll
      for (int k = 0; k < 2; ++k) glds1c(l16, ws + HX_OFF + (c * 32 + 2 * (4 * ((j + d) & 3) + w) + k) * 1024, smb + K2_HF + (8 * d + 2 * w + k) * 1024);
    RLOADS(40, 56);
    asm volatile("s_waitcnt vmcnt(16)" ::: "memory");
    BAR();
    STAMP(4);
    {
      const v4u ones = ONES4;
      f32x16 ag = zero16(), as = zero16();
#pragma unroll
      for (int u = 0; u < 8; ++u) {
        const v4u hb = *(LAS v4u*)(hf + (8 * w + u) * 1024);
        ag = mfma(hb, hb, ag);
        as = mfma(ones, hb, as);
      }
      const float dg = diag16(ag, lane);
      if (hh == 0) *(LAS f32x2*)(sm + K2_ST + r * 32 + w * 8) = f32x2{as[0], dg};
    }
    RLOADS(56, 32 + RD);
    f32x16 acc = mfma32(hh ? 0.f : blo(bb2), 1.0f, zero16());
#pragma unroll
    for (int e = 0; e < 4; ++e) { acc[2 * e] += blo(hf0[e]); acc[2 * e + 1] += bhi(hf0[e]); acc[8 + 2 * e] += blo(hf1[e]); acc[9 + 2 * e] += bhi(hf1[e]); }
    LGKM0();
    BAR();
    BAR();
    STAMP(5);
#define CSTEPS(lo, hi) do { v4u bq_[4]; _Pragma("unroll") for (int p_ = (lo); p_ < (lo) + 3; ++p_) bq_[p_ & 3] = *(LAS v4u*)(h1 + p_ * 1024); \
    _Pragma("unroll") for (int p_ = (lo); p_ < (hi); ++p_) { if (p_ + 3 < (hi)) bq_[(p_ + 3) & 3] = *(LAS v4u*)(h1 + (p_ + 3) * 1024); \
      acc = mfma(ring[(32 + p_) % RD], bq_[p_ & 3], acc); RLOAD(32 + p_ + RD); SB(); } } while (0)
#define PUB(T, pos) do { __builtin_amdgcn_raw_buffer_store_b128(*(LAS v4u*)(h1 + (pos) * 1024), rs, l16, H1X_OFF + (c * 86 + 2 * (T)) * 1024, 16); \
    __builtin_amdgcn_raw_buffer_store_b128(*(LAS v4u*)(h1 + ((pos) + 1) * 1024), rs, l16, H1X_OFF + (c * 86 + 2 * (T) + 1) * 1024, 16); } while (0)
#define GATHER(st) _Pragma("unroll") for (int d = 1; d < 4; ++d) _Pragma("unroll") for (int k = 0; k < 2; ++k) \
    glds1c(l16, ws + H1X_OFF + (c * 86 + 2 * (16 * (st) + 4 * ((j + d) & 3) + w) + k) * 1024, smb + K2_H1 + (8 + 24 * (st) + 8 * d + 2 * w + k) * 1024)
    PUB(q, 2 * w);
    VM0();
    SETFLAG(fl + 16384 + j * 16);
    CSTEPS(0, 8);
    BAR();
    STAMP(6);
    PUB(q + 16, 8 + 2 * w);
    wait_flags(fl + 16384, j, lane);
    GATHER(0);
    VM0();
    SETFLAG(fl + 32768 + j * 16);
    CSTEPS(8, 16);
    BAR();
    STAMP(7);
    if (three) PUB(32 + 4 * w + j, 64 + 2 * (4 * w + j));
    VM0();
    SETFLAG(fl + 49152 + j * 16);
    wait_flags(fl + 32768, j, lane);
    GATHER(1);
    CSTEPS(16, 40);
    VM0();
    BAR();
    STAMP(8);
    wait_flags(fl + 49152, j, lane);
#pragma unroll
    for (int d = 1; d < 4; ++d) {
      const int e = 4 * w + ((j + d) & 3);
      if (e < 11) {
        glds1c(l16, ws + H1X_OFF + (c * 86 + 64 + 2 * e) * 1024, smb + K2_H1 + (64 + 2 * e) * 1024);
        glds1c(l16, ws + H1X_OFF + (c * 86 + 65 + 2 * e) * 1024, smb + K2_H1 + (65 + 2 * e) * 1024);
      }
    }
    CSTEPS(40, 64);
    VM0();
    BAR();
    STAMP(9);
    CSTEPS(64, 86);
    STAMP(10);
#pragma unroll
    for (int k = 0; k < 2; ++k) {
      const v4u o = pack8(acc, k);
      auto r0 = __builtin_amdgcn_permlane32_swap(o[0], o[2], false, false);
      auto r1 = __builtin_amdgcn_permlane32_swap(o[1], o[3], false, false);
      *(GAS v4u*)((char*)out + (size_t)(32 * c + r) * 1024 + (32 * q + 16 * k + 8 * hh) * 2) = v4u{r0[0], r1[0], r0[1], r1[1]};
    }
#undef RLOAD
#undef RLOADS
  } else {
    constexpr int RD = RDB, NTOT = 96;
    const int T2 = three ? (32 + 4 * w + j) : q;
    float csa[3], csb[3]; unsigned bb1[3];
#pragma unroll
    for (int t = 0; t < 3; ++t) {
      const int T = t < 2 ? q + 16 * t : T2;
      const GAS float* cp = (const GAS float*)(ws + CS1_OFF) + T * 64 + r;
      csa[t] = cp[0]; csb[t] = cp[32]; bb1[t] = b1[32 * T + r];
    }
    v4u pf[4][2];
#pragma unroll
    for (int hq = 0; hq < 4; ++hq)
#pragma unroll
      for (int s = 0; s < 2; ++s) pf[hq][s] = __builtin_amdgcn_raw_buffer_load_b128(rs, l16, QF_OFF + ((c * 32 + 4 * wv + hq) * 2 + s) * 1024, 0);
    v4u ring[RD];
    const int sB = W1T_OFF + q * 98304;
    const int l3 = three ? l16 : OOB;
#define RLOAD(i) do { if ((i) < NTOT) ring[(i) % RD] = __builtin_amdgcn_raw_buffer_load_b128(rs, ((i) < 24 ? (i) % 3 == 2 : (i) >= 72) ? l3 : l16, sB + (i) * 1024, 0); } while (0)
#define RLOADS(lo, hi) _Pragma("unroll") for (int i_ = (lo); i_ < (hi); ++i_) RLOAD(i_)
    SB();
    STAMP(1);
    o_phase(pf, rs, sm, b, wv, lane, tstart);
    float ab1[3];
#pragma unroll
    for (int t = 0; t < 3; ++t) ab1[t] = hh ? blo(bb1[t]) : csa[t] + csb[t];
    asm volatile("" : "+v"(ab1[0]), "+v"(ab1[1]), "+v"(ab1[2]));
    LGKM0();
    BAR();
    STAMP(2);
    RLOADS(0, RD);
    BAR();
    STAMP(3);
    f32x16 ac[3];
#pragma unroll
    for (int t = 0; t < 3; ++t) ac[t] = zero16();
    v4u hq2[2];
    hq2[0] = *(LAS v4u*)(hf);
#pragma unroll
    for (int p = 0; p < 8; ++p) {
      const v4u hb = hq2[p & 1];
      if (p < 7) hq2[(p + 1) & 1] = *(LAS v4u*)(hf + (p + 1) * 1024);
#pragma unroll
      for (int t = 0; t < 3; ++t) { ac[t] = mfma(ring[(3 * p + t) % RD], hb, ac[t]); RLOAD(3 * p + t + RD); SB(); }
    }
    BAR();
    STAMP(4);
#define BREST(t) do { v4u bq_[4]; _Pragma("unroll") for (int p_ = 8; p_ < 11; ++p_) bq_[p_ & 3] = *(LAS v4u*)(hf + p_ * 1024); \
    _Pragma("unroll") for (int p_ = 8; p_ < 32; ++p_) { if (p_ + 3 < 32) bq_[(p_ + 3) & 3] = *(LAS v4u*)(hf + (p_ + 3) * 1024); \
      ac[t] = mfma(ring[(16 + 24 * (t) + p_) % RD], bq_[p_ & 3], ac[t]); RLOAD(16 + 24 * (t) + p_ + RD); SB(); } } while (0)
#define FINISH(t, pos_) do { ac[t] = mfma32(ab1[t], bop, ac[t]); \
    _Pragma("unroll") for (int e = 0; e < 16; ++e) { const float p_ = vmax(ac[t][e], 0.f); ac[t][e] = (p_ * p_) * r2; } \
    *(LAS v4u*)(h1 + (pos_) * 1024) = pack8(ac[t], 0); *(LAS v4u*)(h1 + ((pos_) + 1) * 1024) = pack8(ac[t], 1); } while (0)
    BREST(0);
    BAR();
    float bop, r2;
    {
      LAS f32x4* sp = (LAS f32x4*)(sm + K2_ST + r * 32);
      const f32x4 st = sp[0] + sp[1];
      const float mean = (st[0] + st[2]) * (1.0f / 512.0f);
      const float var = vmax((st[1] + st[3]) * (1.0f / 512.0f) - mean * mean, 0.f) + 1e-5f;
      const float rstd = __builtin_amdgcn_rsqf(var);
      bop = hh ? var * rstd : -mean;
      r2 = rstd * rstd;
    }
    FINISH(0, 2 * w);
    LGKM0();
    BAR();
    STAMP(5);
    BREST(1);
    FINISH(1, 8 + 2 * w);
    LGKM0();
    BAR();
    STAMP(6);
    if (three) {
      BREST(2);
      FINISH(2, 64 + 2 * (4 * w + j));
    }
    LGKM0();
    BAR();
    STAMP(7);
    BAR();
    BAR();
#undef RLOAD
#undef RLOADS
  }
  STAMP(11); STAMP_FLUSH(1);
}
}

extern "C" void kernel_launch(void* const* d_in, const int* in_sizes, int n_in, void* d_out, int out_size, void* d_ws, size_t ws_size,
                              hipStream_t stream) {
  const unsigned short* x = (const unsigned short*)d_in[0];
  const unsigned short* wqkv = (const unsigned short*)d_in[1];
  const unsigned short* wphi = (const unsigned short*)d_in[2];
  const unsigned short* wo = (const unsigned short*)d_in[3];
  const unsigned short* w1 = (const unsigned short*)d_in[5];
  const unsigned short* b1 = (const unsigned short*)d_in[6];
  const unsigned short* w2 = (const unsigned short*)d_in[7];
  const unsigned short* b2 = (const unsigned short*)d_in[8];
  char* ws = (char*)d_ws;
  (void)hipFuncSetAttribute((const void*)k1, hipFuncAttributeMaxDynamicSharedMemorySize, K1_LDS);
  (void)hipFuncSetAttribute((const void*)k2, hipFuncAttributeMaxDynamicSharedMemorySize, K2_LDS);
  k1<<<256, 512, K1_LDS, stream>>>(x, wqkv, wphi, wo, w1, w2, ws);
  k2<<<256, 512, K2_LDS, stream>>>(x, b1, b2, ws, (unsigned short*)d_out);
}
```

```cpp
#include <hip/hip_runtime.h>
#include <hip/hip_bf16.h>
#include <stdint.h>

#define GAS __attribute__((address_space(1)))
#define LAS __attribute__((address_space(3)))
#define DI __device__ __forceinline__
typedef unsigned v4u __attribute__((ext_vector_type(4)));
typedef unsigned v2u __attribute__((ext_vector_type(2)));
typedef float f32x16 __attribute__((ext_vector_type(16)));
typedef float f32x4 __attribute__((ext_vector_type(4)));
typedef float f32x2 __attribute__((ext_vector_type(2)));
typedef __bf16 bf16x8 __attribute__((ext_vector_type(8)));
typedef __bf16 bf16x2 __attribute__((ext_vector_type(2)));
typedef short s16x4 __attribute__((ext_vector_type(4)));
typedef GAS unsigned gu32;

namespace {
constexpr int WOT_OFF = 0;
constexpr int W1T_OFF = 512 << 10;
constexpr int W2T_OFF = 2048 << 10;
constexpr int QF_OFF = 16 << 20;
constexpr int KVP_OFF = 6 << 20;
constexpr int KVF_OFF = (7 << 20) + (128 << 10);
constexpr int KVF_REP = 36864;
constexpr int CS1_OFF = (7 << 20) + (640 << 10);
constexpr int WPF_OFF = (7 << 20) + (704 << 10);
constexpr int DUMMY_OFF = (7 << 20) + (768 << 10);
constexpr int HX_OFF = 8 << 20;
constexpr int H1X_OFF = 10 << 20;
constexpr int FLG_OFF = (15 << 20) + (512 << 10);
constexpr int WS_BYTES = 20 << 20;
constexpr unsigned SENT = 0xFFFFFFFFu;

DI f32x16 mfma(v4u a, v4u b, f32x16 c) {
  return __builtin_amdgcn_mfma_f32_32x32x16_bf16(__builtin_bit_cast(bf16x8, a), __builtin_bit_cast(bf16x8, b), c, 0, 0, 0);
}
DI f32x16 mfma32(float a, float b, f32x16 c) { return __builtin_amdgcn_mfma_f32_32x32x2f32(a, b, c, 0, 0, 0); }
DI unsigned pk2(float a, float b) { bf16x2 v; v[0] = (__bf16)a; v[1] = (__bf16)b; return __builtin_bit_cast(unsigned, v); }
DI float blo(unsigned v) { return __uint_as_float(v << 16); }
DI float bhi(unsigned v) { return __uint_as_float(v & 0xffff0000u); }
DI v4u pack8(const f32x16& x, int s) {
  return v4u{pk2(x[8 * s + 0], x[8 * s + 1]), pk2(x[8 * s + 2], x[8 * s + 3]), pk2(x[8 * s + 4], x[8 * s + 5]), pk2(x[8 * s + 6], x[8 * s + 7])};
}
DI v4u nosent(v4u v) {
#pragma unroll
  for (int i = 0; i < 4; ++i) v[i] = (v[i] == SENT) ? 0xFFFEFFFEu : v[i];
  return v;
}
DI bool hassent(v4u v) { return (v[0] == SENT) | (v[1] == SENT) | (v[2] == SENT) | (v[3] == SENT); }
DI float xsum(float v) { auto r = __builtin_amdgcn_permlane32_swap(__float_as_uint(v), __float_as_uint(v), false, false); return __uint_as_float(r[0]) + __uint_as_float(r[1]); }
DI float vmax(float a, float b) { return __builtin_elementwise_maximum(a, b); }
DI v2u trread(LAS char* p) { return __builtin_bit_cast(v2u, __builtin_amdgcn_ds_read_tr16_b64_v4i16((LAS s16x4*)p)); }
DI v4u tr2(LAS char* a, int d) { v2u l = trread(a), h = trread(a + d); return v4u{l[0], l[1], h[0], h[1]}; }
DI f32x16 zero16() { f32x16 z; for (int i = 0; i < 16; ++i) z[i] = 0.f; return z; }
DI float diag16(const f32x16& d, int lane) {
  const int r = lane & 31, hh = lane >> 5;
  const int istar = (r & 3) + 4 * (r >> 3);
  float v = 0.f;
#pragma unroll
  for (int i = 0; i < 16; ++i) v = (i == istar) ? d[i] : v;
  v = (hh == ((r >> 2) & 1)) ? v : 0.f;
  return xsum(v);
}
DI void dual_softmax_sc(f32x16& p, float sc) {
#pragma unroll
  for (int hf = 0; hf < 2; ++hf) {
    float m = vmax(vmax(vmax(p[8 * hf], p[8 * hf + 1]), vmax(p[8 * hf + 2], p[8 * hf + 3])), vmax(vmax(p[8 * hf + 4], p[8 * hf + 5]), vmax(p[8 * hf + 6], p[8 * hf + 7])));
    { auto rr = __builtin_amdgcn_permlane32_swap(__float_as_uint(m), __float_as_uint(m), false, false); m = vmax(__uint_as_float(rr[0]), __uint_as_float(rr[1])); }
    m = -(m * sc);
    float s = 0.f;
#pragma unroll
    for (int i = 0; i < 8; ++i) { float e = __builtin_amdgcn_exp2f(__builtin_fmaf(p[8 * hf + i], sc, m)); p[8 * hf + i] = e; s += e; }
    s = xsum(s);
    const float inv = __builtin_amdgcn_rcpf(s);
#pragma unroll
    for (int i = 0; i < 8; ++i) p[8 * hf + i] *= inv;
  }
}
DI void glds3(unsigned voff, const void* sbase, unsigned lds_dst) {
  unsigned keep;
  asm volatile("s_mov_b32 %0, m0\n\ts_mov_b32 m0, %3\n\ts_nop 0\n\tglobal_load_lds_dwordx4 %1, %2\n\tglobal_load_lds_dwordx4 %1, %2 offset:1024\n\t"
               "global_load_lds_dwordx4 %1, %2 offset:2048\n\ts_mov_b32 m0, %0" : "=&s"(keep) : "v"(voff), "s"(sbase), "s"(lds_dst) : "memory");
}
DI void glds1(unsigned voff, const void* sbase, unsigned lds_dst) {
  unsigned keep;
  asm volatile("s_mov_b32 %0, m0\n\ts_mov_b32 m0, %3\n\ts_nop 0\n\tglobal_load_lds_dwordx4 %1, %2\n\ts_mov_b32 m0, %0" : "=&s"(keep) : "v"(voff), "s"(sbase), "s"(lds_dst) : "memory");
}
DI void glds1c(unsigned voff, const void* sbase, unsigned lds_dst) {
  unsigned keep;
  asm volatile("s_mov_b32 %0, m0\n\ts_mov_b32 m0, %3\n\ts_nop 0\n\tglobal_load_lds_dwordx4 %1, %2 sc1\n\ts_mov_b32 m0, %0" : "=&s"(keep) : "v"(voff), "s"(sbase), "s"(lds_dst) : "memory");
}
#define SB() __builtin_amdgcn_sched_barrier(0)
#define LGKM0() asm volatile("s_waitcnt lgkmcnt(0)" ::: "memory")
#define BAR() __builtin_amdgcn_s_barrier()
#define ONES4 v4u{0x3f803f80u, 0x3f803f80u, 0x3f803f80u, 0x3f803f80u}
DI int pos_c(int s, int j) {
  const int T = s >> 1;
  if (T >= 32) return s;
  const int st = T >> 4, d = (((T >> 2) & 3) - j) & 3, lo = 2 * (T & 3) + (s & 1);
  return d == 0 ? 8 * st + lo : 8 + 24 * st + 8 * d + lo;
}

constexpr int K1_WL = 0;
constexpr int K1_PK = 0;
constexpr int K1_V = 16384;
constexpr int K1_WV = 98304;
constexpr int K1_CS = 147456;
constexpr int K1_MS = 150528;
constexpr int K1_C1 = 154624;
constexpr int K1_LDS = 156672;
constexpr unsigned CODE_SPAN = 25000;

__global__ __launch_bounds__(512, 2) void k1(const unsigned short* __restrict__ x, const unsigned short* __restrict__ wqkv,
                                             const unsigned short* __restrict__ wphi, const unsigned short* __restrict__ wo,
                                             const unsigned short* __restrict__ w1, const unsigned short* __restrict__ w2, char* __restrict__ ws) {
  extern __shared__ __attribute__((aligned(16))) char sm_[];
  LAS char* sm = (LAS char*)sm_;
  const unsigned smb = (unsigned)(size_t)sm_;
  const int lane = threadIdx.x & 63, wv = __builtin_amdgcn_readfirstlane(threadIdx.x >> 6);
  const int tg = wv & 3, KH = wv >> 2;
  const int r = lane & 31, hh = lane >> 5;
  const int b = blockIdx.x, xcd = b & 7, bi = b >> 3;
  const int tt = 4 * (xcd >> 1) + (bi & 3), g = 8 * (xcd & 1) + (bi >> 2);
  const int row8 = lane >> 3, part = lane & 7, p4 = lane & 3;
  const __amdgpu_buffer_rsrc_t rs = __builtin_amdgcn_make_buffer_rsrc((void*)ws, 0, WS_BYTES, 0x00020000);
  const int l16 = lane * 16;
  v4u xr[16];
  const unsigned xvo = row8 * 1024 + part * 16;
  const char* xsb = (const char*)x + (size_t)(tt * 128 + tg * 32) * 1024 + KH * 512;
  const unsigned wvo = (lane >> 2) * 3072 + p4 * 16;
  const char* wsb = (const char*)wqkv + (size_t)(256 * KH + 16 * tg) * 3072 + g * 64;
  const unsigned wld = smb + K1_WL + ((16 * KH + tg) * 3) * 1024;
#define XLOAD1(R, rg) asm volatile("global_load_dwordx4 %0, %1, %2 offset:%3" : "=v"(xr[4 * (R) + (rg)]) : "v"(xvo), "s"(xsb + (rg) * 8192), "n"((R) * 128) : "memory")
#define XLOAD(R) do { XLOAD1(R, 0); XLOAD1(R, 1); XLOAD1(R, 2); XLOAD1(R, 3); } while (0)
#define WLOAD(R) glds3(wvo, wsb + (size_t)(R) * 64 * 3072, wld + (R) * 12288)
  XLOAD(0); WLOAD(0); XLOAD(1);
  {
    const unsigned long long pc = __builtin_amdgcn_s_getpc() & ~255ull;
    unsigned off = (unsigned)threadIdx.x * 64u;
    off = off < CODE_SPAN ? off : CODE_SPAN;
    unsigned keep;
    asm volatile("s_mov_b32 %0, m0\n\ts_mov_b32 m0, %3\n\ts_nop 0\n\tglobal_load_lds_dword %1, %2\n\ts_mov_b32 m0, %0" : "=&s"(keep) : "v"(off), "s"(pc), "s"(smb + K1_C1 + wv * 256) : "memory");
    const char* tb; unsigned to;
    if (wv < 2) { tb = (const char*)x + (size_t)(xcd >> 1) * 524288 + bi * 16384 + wv * 8192; to = lane * 128; }
    else { const int e = (wv < 5 ? wv - 2 : 0) * 64 + lane; tb = (const char*)wqkv + (size_t)bi * 49152 + (xcd & 1) * 512; to = (e / 12) * 3072 + ((e % 12) >> 2) * 1024 + (e & 3) * 128; }
    asm volatile("s_mov_b32 %0, m0\n\ts_mov_b32 m0, %3\n\ts_nop 0\n\tglobal_load_lds_dword %1, %2\n\ts_mov_b32 m0, %0" : "=&s"(keep) : "v"(to), "s"(tb), "s"(smb + K1_C1 + wv * 256) : "memory");
  }
  const bool isw1 = b < 86;
  int rt_dst[2];
  f32x16 aq = zero16(), ak = zero16(), av = zero16(), c0 = zero16(), c1 = zero16(), c2 = zero16();
  const v4u ones = ONES4;
  float sa = 0.f, sb = 0.f, qa = 0.f, qb = 0.f;
  const int bl = (lane >> 4) & 1, q4 = (lane & 15) >> 2;
  LAS char* ap0 = sm + K1_WL + (16 * KH) * 3072 + (8 * hh + q4) * 64 + (bl ^ KH) * 32 + p4 * 8;
  LAS char* ap[4] = {ap0 + tg * 3072, ap0 + (tg ^ 1) * 3072, ap0 + (tg ^ 2) * 3072, ap0 + (tg ^ 3) * 3072};
  LAS char* stg = sm + K1_WV + wv * 6144;
  LAS char* stw0 = stg + row8 * 128 + ((part ^ (row8 >> 1)) << 4);
  LAS char* stw1 = stg + row8 * 128 + ((part ^ (row8 >> 1) ^ 4) << 4);
  const unsigned srd = (r * 128 + ((hh ^ ((r >> 1) & 7)) << 4)) ^ (tg << 5);
#define XWAIT(R, N) asm volatile("s_waitcnt vmcnt(%4)" : "+v"(xr[4 * (R)]), "+v"(xr[4 * (R) + 1]), "+v"(xr[4 * (R) + 2]), "+v"(xr[4 * (R) + 3]) : "n"(N) : "memory")
#define STW(R, k) *(LAS v4u*)(((k) & 1 ? stw1 : stw0) + (k) * 1024) = xr[4 * (R) + (k)]
#define STR(XF, k) XF[k] = *(LAS v4u*)(stg + (srd ^ ((k) << 5)))
#define STAT(v) do { const float lo_ = blo(v), hi_ = bhi(v); sa += lo_; sb += hi_; qa = __builtin_fmaf(lo_, lo_, qa); qb = __builtin_fmaf(hi_, hi_, qb); \
    asm volatile("" : "+v"(sa), "+v"(sb), "+v"(qa), "+v"(qb)); } while (0)
  v4u xf[2][4];
  XWAIT(0, 9);
  STW(0, 0); STW(0, 1); STW(0, 2); STW(0, 3); STR(xf[0], 0); STR(xf[0], 1); STR(xf[0], 2); STR(xf[0], 3);
  asm volatile("s_waitcnt vmcnt(6)" ::: "memory");
#pragma unroll
  for (int rho = 0; rho < 4; ++rho) {
    BAR();
    v4u f[2][3];
#pragma unroll
    for (int c = 0; c < 3; ++c) f[0][c] = tr2(ap[0] + rho * 12288 + c * 1024, 256);
    SB();
#pragma unroll
    for (int u = 0; u < 4; ++u) {
      const v4u xb = xf[rho & 1][u];
#pragma unroll
      for (int c = 0; c < 3; ++c) {
        if (c == 0) aq = mfma(f[u & 1][0], xb, aq);
        if (c == 1) ak = mfma(f[u & 1][1], xb, ak);
        if (c == 2) av = mfma(f[u & 1][2], xb, av);
        if (u < 3) f[(u + 1) & 1][c] = tr2(ap[u + 1] + rho * 12288 + c * 1024, 256);
        STAT(xb[c]);
        if (rho == 0 && u == 0 && c == 0) WLOAD(1);
        if (rho == 0 && u == 1 && c == 1) WLOAD(2);
        if (rho == 1 && u == 1 && c == 0) WLOAD(3);
        if (rho < 2 && u == 0 && c == 1) { XLOAD1(rho + 2, 0); XLOAD1(rho + 2, 1); }
        if (rho < 2 && u == 0 && c == 2) { XLOAD1(rho + 2, 2); XLOAD1(rho + 2, 3); }
        if (rho == 1 && u == 1 && c == 2) {
#pragma unroll
          for (int u2 = 0; u2 < 2; ++u2) {
            const int i = 2 * wv + u2;
            const char* src; int dst;
            if (isw1) {
              const int T = b >> 1, s = 16 * (b & 1) + i, e = T - 32;
              const int q = T < 32 ? (T & 15) : (4 * (e & 3) + (e >> 2)), t = T < 32 ? (T >> 4) : 2;
              const int p = ((((s >> 3) - (q >> 2)) & 3) << 3) + (s & 7);
              const int m = p < 8 ? 3 * p + t : 16 + 24 * t + p;
              src = (const char*)w1 + (size_t)s * 44032 + T * 64;
              dst = W1T_OFF + (q * 96 + m) * 1024;
            } else {
              int id = (b - 86) * 12 + i;
              const bool ok = (i < 12) && (id < 1888);
              id = ok ? id : 0;
              const int sb = id >> 4, q = id & 15;
              if (sb < 32) { src = (const char*)wo + sb * 16384 + q * 64; dst = WOT_OFF + (q * 32 + sb) * 1024; }
              else { src = (const char*)w2 + (sb - 32) * 16384 + q * 64; dst = W2T_OFF + (q * 86 + pos_c(sb - 32, q >> 2)) * 1024; }
              if (!ok) dst = DUMMY_OFF;
            }
            rt_dst[u2] = dst;
            glds1((lane >> 2) * (isw1 ? 2752 : 1024) + p4 * 16, src, smb + K1_WV + wv * 6144 + 4096 + u2 * 1024);
          }
        }
        if (rho < 3 && u == 2 && c == 0) { XWAIT(rho + 1, rho == 2 ? 5 : rho == 0 ? 12 : 12); STW(rho + 1, 0); }
        if (rho < 3 && u == 2 && c == 1) { STW(rho + 1, 1); }
        if (rho < 3 && u == 2 && c == 2) { STW(rho + 1, 2); }
        if (rho < 3 && u == 3 && c == 0) { STW(rho + 1, 3); }
        if (rho < 3 && u == 3 && c == 1) { STR(xf[(rho + 1) & 1], 0); STR(xf[(rho + 1) & 1], 1); }
        if (rho < 3 && u == 3 && c == 2) { STR(xf[(rho + 1) & 1], 2); STR(xf[(rho + 1) & 1], 3); }
        SB();
        if (u == 0) {
          if (c == 0) c0 = mfma(ones, f[0][0], c0);
          if (c == 1) c1 = mfma(ones, f[0][1], c1);
          if (c == 2) c2 = mfma(ones, f[0][2], c2);
          if (c == 2) STAT(xb[3]);
          SB();
        }
      }
      if (u > 0) { STAT(xb[3]); SB(); }
    }
    if (rho < 3) asm volatile("s_waitcnt vmcnt(%0)" :: "n"(rho == 2 ? 2 : rho == 0 ? 7 : 9) : "memory");
  }
  unsigned we[8];
  {
    asm volatile("s_waitcnt vmcnt(0)" ::: "memory");
    LAS char* ra = stg + 4096 + (4 * hh + q4) * 64 + p4 * 8;
    const v4u f0 = tr2(ra + bl * 32, 512), f1 = tr2(ra + 1024 + bl * 32, 512);
    __builtin_amdgcn_raw_buffer_store_b128(f0, rs, l16, rt_dst[0], 0);
    __builtin_amdgcn_raw_buffer_store_b128(f1, rs, l16, rt_dst[1], 0);
#pragma unroll
    for (int j = 0; j < 8; ++j) we[j] = wphi[((2 * g + KH) * 16 + 8 * (j >> 2) + 4 * hh + (j & 3)) * 16 + (r & 15)];
    if (isw1) {
      f32x16 ca = mfma(ones, f0, zero16());
      ca = mfma(ones, f1, ca);
      if (hh == 0) ((LAS float*)(sm + K1_C1))[wv * 32 + r] = ca[0];
    }
  }
  *(LAS f32x2*)(sm + K1_MS + (tg * 32 + r) * 32 + KH * 16 + hh * 8) = f32x2{sa + sb, qa + qb};
  if (hh == 0) { LAS float* cs = (LAS float*)(sm + K1_CS) + (r ^ (16 * KH)) * 8 + wv; cs[0] = c0[0]; cs[256] = c1[0]; cs[512] = c2[0]; }
  {
    LAS f32x4* ex = (LAS f32x4*)(stg) + lane;
    ex[0] = f32x4{aq[8], aq[9], aq[10], aq[11]}; ex[64] = f32x4{aq[12], aq[13], aq[14], aq[15]};
    ex[128] = f32x4{ak[8], ak[9], ak[10], ak[11]}; ex[192] = f32x4{ak[12], ak[13], ak[14], ak[15]};
    ex[256] = f32x4{av[8], av[9], av[10], av[11]}; ex[320] = f32x4{av[12], av[13], av[14], av[15]};
  }
  LGKM0();
  BAR();
  float rstd;
  {
    LAS f32x4* ex = (LAS f32x4*)(sm + K1_WV + (wv ^ 4) * 6144) + lane;
    LAS f32x4* sp = (LAS f32x4*)(sm + K1_MS + (tg * 32 + r) * 32);
    LAS f32x4* cp = (LAS f32x4*)(sm + K1_CS + (r ^ (16 * KH)) * 32);
    const f32x4 st = sp[0] + sp[1];
    const f32x4 cb = cp[64] + cp[65];
    const f32x4 e2 = ex[128], e3 = ex[192];
    const float mean = (st[0] + st[2]) * (1.0f / 512.0f);
    const float hz = hh ? 0.f : 1.f;
    const float cs1 = ((cb[0] + cb[1]) + (cb[2] + cb[3])) * hz;
#pragma unroll
    for (int e = 0; e < 4; ++e) { ak[e] += e2[e]; ak[4 + e] += e3[e]; }
    ak = mfma32(cs1, -mean, ak);
    const f32x4 ca = cp[0] + cp[1], cc = cp[128] + cp[129];
    const f32x4 e0 = ex[0], e1 = ex[64], e4 = ex[256], e5 = ex[320];
    const float cs0 = ((ca[0] + ca[1]) + (ca[2] + ca[3])) * hz, cs2 = ((cc[0] + cc[1]) + (cc[2] + cc[3])) * hz;
#pragma unroll
    for (int e = 0; e < 4; ++e) { aq[e] += e0[e]; aq[4 + e] += e1[e]; av[e] += e4[e]; av[4 + e] += e5[e]; }
    const float var = vmax((st[1] + st[3]) * (1.0f / 512.0f) - mean * mean, 0.f);
    rstd = __builtin_amdgcn_rsqf(var + 1e-5f);
    av = mfma32(cs2, -mean, av);
    aq = mfma32(cs0, -mean, aq);
  }
  const unsigned sg = (r >= 16) ? 0x80008000u : 0u;
  const v4u wp = v4u{(we[0] | (we[1] << 16)) ^ sg, (we[2] | (we[3] << 16)) ^ sg, (we[4] | (we[5] << 16)) ^ sg, (we[6] | (we[7] << 16)) ^ sg};
  f32x16 pk = mfma(wp, pack8(ak, 0), zero16());
  {
    LAS char* vi = sm + K1_V + tg * 2048 + r * 64 + KH * 32 + hh * 8;
    *(LAS v2u*)(vi) = v2u{pk2(av[0] * rstd, av[1] * rstd), pk2(av[2] * rstd, av[3] * rstd)};
    *(LAS v2u*)(vi + 16) = v2u{pk2(av[4] * rstd, av[5] * rstd), pk2(av[6] * rstd, av[7] * rstd)};
  }
  dual_softmax_sc(pk, rstd * 1.4426950408889634f);
  {
    LAS char* pi = sm + K1_PK + (tg * 2 + KH) * 2048 + r * 64 + hh * 8;
#pragma unroll
    for (int gq = 0; gq < 4; ++gq) *(LAS v2u*)(pi + gq * 16) = v2u{pk2(pk[4 * gq], pk[4 * gq + 1]), pk2(pk[4 * gq + 2], pk[4 * gq + 3])};
  }
  LGKM0();
  BAR();
  if (tg == 0) {
    f32x16 z = zero16();
    const bool isv = ((r >> 4) == KH);
    LAS char* pa = sm + K1_PK + KH * 2048 + (8 * hh + q4) * 64 + bl * 32 + p4 * 8;
    LAS char* pv = sm + K1_V + (8 * hh + q4) * 64 + bl * 32 + p4 * 8;
#pragma unroll
    for (int ks = 0; ks < 8; ++ks) {
      const v4u a = tr2(pa + (ks >> 1) * 4096 + (ks & 1) * 1024, 256);
      v4u bv = tr2(pv + ks * 1024, 256);
      if (!isv) bv = ones;
      z = mfma(a, bv, z);
    }
    if (isv || ((r & 15) == 0)) {
      GAS f32x4* dst = (GAS f32x4*)(ws + KVP_OFF + (size_t)(tt * 32 + 2 * g + KH) * 2176 + (isv ? (r & 15) : 16) * 128 + hh * 64);
#pragma unroll
      for (int gq = 0; gq < 4; ++gq) dst[gq] = f32x4{z[4 * gq], z[4 * gq + 1], z[4 * gq + 2], z[4 * gq + 3]};
    }
  } else if (tg == 1) {
    if (KH == 0 && isw1 && lane < 32) {
      LAS float* cp = (LAS float*)(sm + K1_C1) + lane;
      float s = 0.f;
#pragma unroll
      for (int k = 0; k < 8; ++k) s += cp[32 * k];
      ((GAS float*)(ws + CS1_OFF))[b * 32 + lane] = s;
    }
    if (KH == 1) { __builtin_amdgcn_raw_buffer_store_b128(v4u{SENT, SENT, SENT, SENT}, rs, l16, KVF_OFF + b * 1024, 0); if (b < 32) __builtin_amdgcn_raw_buffer_store_b128(v4u{SENT, SENT, SENT, SENT}, rs, l16, KVF_OFF + (256 + b) * 1024, 0); }
    if (KH == 1 && lane < 16) ((gu32*)(ws + FLG_OFF))[(b * 16 + lane) * 16] = 0u;
  }
  {
    f32x16 pq = mfma(wp, pack8(aq, 0), zero16());
    dual_softmax_sc(pq, rstd * 1.4426950408889634f);
    const int qo = QF_OFF + ((tt * 4 + tg) * 32 + 2 * g + KH) * 2048;
    __builtin_amdgcn_raw_buffer_store_b128(pack8(pq, 0), rs, l16, qo, 0);
    __builtin_amdgcn_raw_buffer_store_b128(pack8(pq, 1), rs, l16, qo + 1024, 0);
  }
}

constexpr int K2_OF = 0;
constexpr int K2_HF = 32768;
constexpr int K2_H1 = 65536;
constexpr int K2_ST = 153600;
constexpr int K2_LDS = 154624;
#define KV_GATE 235
constexpr int RDA = 42, RDB = 38;
constexpr int OOB = 0x7FFFFF00;

DI void wait_flags(gu32* f, int j, int lane) {
  const bool need = lane < 4 && lane != j;
#pragma unroll 1
  for (unsigned sp = 0; sp < (1u << 22); ++sp) {
    unsigned v = 1u;
    if (need) v = __hip_atomic_load(f + lane * 16, __ATOMIC_RELAXED, __HIP_MEMORY_SCOPE_AGENT);
    if (__builtin_amdgcn_ballot_w64(v == 0u) == 0ull) break;
    __builtin_amdgcn_s_sleep(1);
  }
  __builtin_amdgcn_fence(__ATOMIC_ACQUIRE, "wavefront");
}
#define VM0() asm volatile("s_waitcnt vmcnt(0)" ::: "memory")
#define SETFLAG(p) do { if (lane == 0) __hip_atomic_store((p), 1u, __ATOMIC_RELAXED, __HIP_MEMORY_SCOPE_AGENT); } while (0)

DI void o_phase(const v4u (&pf)[4][2], __amdgpu_buffer_rsrc_t rs, LAS char* sm, int b, int wv, int lane, unsigned long long tstart) {
  const int r = lane & 31, hh = lane >> 5;
  while (__builtin_amdgcn_s_memrealtime() - tstart < KV_GATE) __builtin_amdgcn_s_sleep(1);
  v4u kv[4][2];
  const int ko = (r < 16) ? r * 64 + hh * 32 : ((r == 16) | (r == 20)) ? 1024 + hh * 32 : OOB;
#pragma unroll 1
  for (unsigned sp = 0; sp < (1u << 22); ++sp) {
    bool bad = false;
#pragma unroll
    for (int hq = 0; hq < 4; ++hq)
#pragma unroll
      for (int s = 0; s < 2; ++s) {
        kv[hq][s] = __builtin_amdgcn_raw_buffer_load_b128(rs, ko + s * 16, KVF_OFF + (b & 7) * KVF_REP + (4 * wv + hq) * 1088, 16);
        bad |= hassent(kv[hq][s]);
      }
    if (__builtin_amdgcn_ballot_w64(bad) == 0ull) break;
    __builtin_amdgcn_s_sleep(4);
  }
#pragma unroll
  for (int hq = 0; hq < 4; ++hq) {
    f32x16 d = mfma(kv[hq][0], pf[hq][0], zero16());
    d = mfma(kv[hq][1], pf[hq][1], d);
    const float inv = __builtin_amdgcn_rcpf(d[8] + 1e-6f);
#pragma unroll
    for (int i = 0; i < 8; ++i) d[i] *= inv;
    *(LAS v4u*)(sm + K2_OF + (4 * wv + hq) * 1024 + lane * 16) = pack8(d, 0);
  }
}

__global__ __launch_bounds__(512, 2) void k2(const unsigned short* __restrict__ x, const unsigned short* __restrict__ b1,
                                             const unsigned short* __restrict__ b2, char* __restrict__ ws, unsigned short* __restrict__ out) {
  extern __shared__ __attribute__((aligned(16))) char sm_[];
  LAS char* sm = (LAS char*)sm_;
  const unsigned smb = (unsigned)(size_t)sm_;
  const int lane = threadIdx.x & 63, wv = __builtin_amdgcn_readfirstlane(threadIdx.x >> 6);
  const int w = wv & 3;
  const int r = lane & 31, hh = lane >> 5;
  const int b = blockIdx.x, c = (b & 7) * 8 + (b >> 5), j = (b >> 3) & 3, q = 4 * j + w;
  const bool three = (4 * w + j) < 11;
  const __amdgpu_buffer_rsrc_t rs = __builtin_amdgcn_make_buffer_rsrc((void*)ws, 0, WS_BYTES, 0x00020000);
  const int l16 = lane * 16;
  const unsigned long long tstart = __builtin_amdgcn_s_memrealtime();
  LAS char* hf = sm + K2_HF + l16;
  LAS char* h1 = sm + K2_H1 + l16;
  if (wv < 4) {
    constexpr int RD = RDA, NTOT = 118;
    const bool red = b < 136;
    f32x4 kp[2] = {{0.f, 0.f, 0.f, 0.f}, {0.f, 0.f, 0.f, 0.f}};
    if (red) {
      const GAS f32x4* pp = (const GAS f32x4*)(ws + KVP_OFF + (size_t)(lane >> 2) * 69632) + (b * 4 + w) * 8 + (lane & 3);
      kp[0] = pp[0]; kp[1] = pp[4];
    }
    v2u xr[4];
#pragma unroll
    for (int g = 0; g < 4; ++g) xr[g] = *(const GAS v2u*)((const char*)x + (size_t)(32 * c + r) * 1024 + (32 * q + 8 * g + 4 * hh) * 2);
    const unsigned bb2 = b2[32 * q + r];
    v4u pf[4][2];
#pragma unroll
    for (int hq = 0; hq < 4; ++hq)
#pragma unroll
      for (int s = 0; s < 2; ++s) pf[hq][s] = __builtin_amdgcn_raw_buffer_load_b128(rs, l16, QF_OFF + ((c * 32 + 4 * wv + hq) * 2 + s) * 1024, 0);
    v4u ring[RD];
    const int sA = WOT_OFF + q * 32768, sC = W2T_OFF + q * 88064 - 32768;
#define RLOAD(i) do { if ((i) < NTOT) ring[(i) % RD] = __builtin_amdgcn_raw_buffer_load_b128(rs, l16, ((i) < 32 ? sA : sC) + (i) * 1024, 0); } while (0)
#define RLOADS(lo, hi) _Pragma("unroll") for (int i_ = (lo); i_ < (hi); ++i_) RLOAD(i_)
    RLOADS(0, 8);
    SB();
    if (red) {
#pragma unroll
      for (int k = 0; k < 2; ++k) {
#pragma unroll
        for (int e = 0; e < 4; ++e) {
          float v = kp[k][e];
          v += __builtin_bit_cast(float, __builtin_amdgcn_update_dpp(0, __builtin_bit_cast(int, v), 0x124, 0xf, 0xf, false));
          v += __builtin_bit_cast(float, __builtin_amdgcn_update_dpp(0, __builtin_bit_cast(int, v), 0x128, 0xf, 0xf, false));
          { auto s2 = __builtin_amdgcn_permlane16_swap(__float_as_uint(v), __float_as_uint(v), false, false); v = __uint_as_float(s2[0]) + __uint_as_float(s2[1]); }
          kp[k][e] = xsum(v);
        }
      }
      const f32x4 kk = (lane & 4) ? kp[1] : kp[0];
      unsigned v0 = pk2(kk[0], kk[1]), v1 = pk2(kk[2], kk[3]);
      v0 = (v0 == SENT) ? 0xFFFEFFFEu : v0; v1 = (v1 == SENT) ? 0xFFFEFFFEu : v1;
      __hip_atomic_store((GAS unsigned long long*)(ws + KVF_OFF + (lane >> 3) * KVF_REP) + (b * 4 + w) * 8 + (lane & 7), (unsigned long long)v0 | ((unsigned long long)v1 << 32), __ATOMIC_RELAXED, __HIP_MEMORY_SCOPE_AGENT);
    }
    RLOADS(8, 16);
    SB();
    o_phase(pf, rs, sm, b, wv, lane, tstart);
    RLOADS(16, 32);
    LGKM0();
    BAR();
    v4u hf0, hf1;
    {
      f32x16 acc;
#pragma unroll
      for (int g = 0; g < 4; ++g) { acc[4 * g] = blo(xr[g][0]); acc[4 * g + 1] = bhi(xr[g][0]); acc[4 * g + 2] = blo(xr[g][1]); acc[4 * g + 3] = bhi(xr[g][1]); }
      LAS char* ob = sm + K2_OF + l16;
      v4u bq[4];
#pragma unroll
      for (int i = 0; i < 3; ++i) bq[i] = *(LAS v4u*)(ob + i * 1024);
#pragma unroll
      for (int i = 0; i < 32; ++i) { if (i + 3 < 32) bq[(i + 3) & 3] = *(LAS v4u*)(ob + (i + 3) * 1024); acc = mfma(ring[i % RD], bq[i & 3], acc); SB(); }
      hf0 = pack8(acc, 0); hf1 = pack8(acc, 1);
    }
    __builtin_amdgcn_raw_buffer_store_b128(hf0, rs, l16, HX_OFF + (c * 32 + 2 * q) * 1024, 16);
    __builtin_amdgcn_raw_buffer_store_b128(hf1, rs, l16, HX_OFF + (c * 32 + 2 * q + 1) * 1024, 16);
    *(LAS v4u*)(hf + (2 * w) * 1024) = hf0;
    *(LAS v4u*)(hf + (2 * w + 1) * 1024) = hf1;
    gu32* fl = (gu32*)(ws + FLG_OFF) + ((c * 4 + w) * 4) * 16;
    VM0();
    SETFLAG(fl + j * 16);
    LGKM0();
    BAR();
    RLOADS(32, 40);
    wait_flags(fl, j, lane);
#pragma unroll
    for (int d = 1; d < 4; ++d)
#pragma unroll
      for (int k = 0; k < 2; ++k) glds1c(l16, ws + HX_OFF + (c * 32 + 2 * (4 * ((j + d) & 3) + w) + k) * 1024, smb + K2_HF + (8 * d + 2 * w + k) * 1024);
    RLOADS(40, 56);
    asm volatile("s_waitcnt vmcnt(16)" ::: "memory");
    BAR();
    {
      const v4u ones = ONES4;
      f32x16 ag = zero16(), as = zero16();
#pragma unroll
      for (int u = 0; u < 8; ++u) {
        const v4u hb = *(LAS v4u*)(hf + (8 * w + u) * 1024);
        ag = mfma(hb, hb, ag);
        as = mfma(ones, hb, as);
      }
      const float dg = diag16(ag, lane);
      if (hh == 0) *(LAS f32x2*)(sm + K2_ST + r * 32 + w * 8) = f32x2{as[0], dg};
    }
    RLOADS(56, 32 + RD);
    f32x16 acc = mfma32(hh ? 0.f : blo(bb2), 1.0f, zero16());
#pragma unroll
    for (int e = 0; e < 4; ++e) { acc[2 * e] += blo(hf0[e]); acc[2 * e + 1] += bhi(hf0[e]); acc[8 + 2 * e] += blo(hf1[e]); acc[9 + 2 * e] += bhi(hf1[e]); }
    LGKM0();
    BAR();
    BAR();
#define CSTEPS(lo, hi) do { v4u bq_[4]; _Pragma("unroll") for (int p_ = (lo); p_ < (lo) + 3; ++p_) bq_[p_ & 3] = *(LAS v4u*)(h1 + p_ * 1024); \
    _Pragma("unroll") for (int p_ = (lo); p_ < (hi); ++p_) { if (p_ + 3 < (hi)) bq_[(p_ + 3) & 3] = *(LAS v4u*)(h1 + (p_ + 3) * 1024); \
      acc = mfma(ring[(32 + p_) % RD], bq_[p_ & 3], acc); RLOAD(32 + p_ + RD); SB(); } } while (0)
#define PUB(T, pos) do { __builtin_amdgcn_raw_buffer_store_b128(*(LAS v4u*)(h1 + (pos) * 1024), rs, l16, H1X_OFF + (c * 86 + 2 * (T)) * 1024, 16); \
    __builtin_amdgcn_raw_buffer_store_b128(*(LAS v4u*)(h1 + ((pos) + 1) * 1024), rs, l16, H1X_OFF + (c * 86 + 2 * (T) + 1) * 1024, 16); } while (0)
#define GATHER(st) _Pragma("unroll") for (int d = 1; d < 4; ++d) _Pragma("unroll") for (int k = 0; k < 2; ++k) \
    glds1c(l16, ws + H1X_OFF + (c * 86 + 2 * (16 * (st) + 4 * ((j + d) & 3) + w) + k) * 1024, smb + K2_H1 + (8 + 24 * (st) + 8 * d + 2 * w + k) * 1024)
    PUB(q, 2 * w);
    VM0();
    SETFLAG(fl + 16384 + j * 16);
    CSTEPS(0, 8);
    BAR();
    PUB(q + 16, 8 + 2 * w);
    wait_flags(fl + 16384, j, lane);
    GATHER(0);
    VM0();
    SETFLAG(fl + 32768 + j * 16);
    CSTEPS(8, 16);
    BAR();
    if (three) PUB(32 + 4 * w + j, 64 + 2 * (4 * w + j));
    VM0();
    SETFLAG(fl + 49152 + j * 16);
    wait_flags(fl + 32768, j, lane);
    GATHER(1);
    CSTEPS(16, 40);
    VM0();
    BAR();
    wait_flags(fl + 49152, j, lane);
#pragma unroll
    for (int d = 1; d < 4; ++d) {
      const int e = 4 * w + ((j + d) & 3);
      if (e < 11) {
        glds1c(l16, ws + H1X_OFF + (c * 86 + 64 + 2 * e) * 1024, smb + K2_H1 + (64 + 2 * e) * 1024);
        glds1c(l16, ws + H1X_OFF + (c * 86 + 65 + 2 * e) * 1024, smb + K2_H1 + (65 + 2 * e) * 1024);
      }
    }
    CSTEPS(40, 64);
    VM0();
    BAR();
    CSTEPS(64, 86);
#pragma unroll
    for (int k = 0; k < 2; ++k) {
      const v4u o = pack8(acc, k);
      auto r0 = __builtin_amdgcn_permlane32_swap(o[0], o[2], false, false);
      auto r1 = __builtin_amdgcn_permlane32_swap(o[1], o[3], false, false);
      *(GAS v4u*)((char*)out + (size_t)(32 * c + r) * 1024 + (32 * q + 16 * k + 8 * hh) * 2) = v4u{r0[0], r1[0], r0[1], r1[1]};
    }
#undef RLOAD
#undef RLOADS
  } else {
    constexpr int RD = RDB, NTOT = 96;
    const int T2 = three ? (32 + 4 * w + j) : q;
    float csa[3], csb[3]; unsigned bb1[3];
#pragma unroll
    for (int t = 0; t < 3; ++t) {
      const int T = t < 2 ? q + 16 * t : T2;
      const GAS float* cp = (const GAS float*)(ws + CS1_OFF) + T * 64 + r;
      csa[t] = cp[0]; csb[t] = cp[32]; bb1[t] = b1[32 * T + r];
    }
    v4u pf[4][2];
#pragma unroll
    for (int hq = 0; hq < 4; ++hq)
#pragma unroll
      for (int s = 0; s < 2; ++s) pf[hq][s] = __builtin_amdgcn_raw_buffer_load_b128(rs, l16, QF_OFF + ((c * 32 + 4 * wv + hq) * 2 + s) * 1024, 0);
    v4u ring[RD];
    const int sB = W1T_OFF + q * 98304;
    const int l3 = three ? l16 : OOB;
#define RLOAD(i) do { if ((i) < NTOT) ring[(i) % RD] = __builtin_amdgcn_raw_buffer_load_b128(rs, ((i) < 24 ? (i) % 3 == 2 : (i) >= 72) ? l3 : l16, sB + (i) * 1024, 0); } while (0)
#define RLOADS(lo, hi) _Pragma("unroll") for (int i_ = (lo); i_ < (hi); ++i_) RLOAD(i_)
    SB();
    o_phase(pf, rs, sm, b, wv, lane, tstart);
    float ab1[3];
#pragma unroll
    for (int t = 0; t < 3; ++t) ab1[t] = hh ? blo(bb1[t]) : csa[t] + csb[t];
    asm volatile("" : "+v"(ab1[0]), "+v"(ab1[1]), "+v"(ab1[2]));
    LGKM0();
    BAR();
    RLOADS(0, RD);
    BAR();
    f32x16 ac[3];
#pragma unroll
    for (int t = 0; t < 3; ++t) ac[t] = zero16();
    v4u hq2[2];
    hq2[0] = *(LAS v4u*)(hf);
#pragma unroll
    for (int p = 0; p < 8; ++p) {
      const v4u hb = hq2[p & 1];
      if (p < 7) hq2[(p + 1) & 1] = *(LAS v4u*)(hf + (p + 1) * 1024);
#pragma unroll
      for (int t = 0; t < 3; ++t) { ac[t] = mfma(ring[(3 * p + t) % RD], hb, ac[t]); RLOAD(3 * p + t + RD); SB(); }
    }
    BAR();
#define BREST(t) do { v4u bq_[4]; _Pragma("unroll") for (int p_ = 8; p_ < 11; ++p_) bq_[p_ & 3] = *(LAS v4u*)(hf + p_ * 1024); \
    _Pragma("unroll") for (int p_ = 8; p_ < 32; ++p_) { if (p_ + 3 < 32) bq_[(p_ + 3) & 3] = *(LAS v4u*)(hf + (p_ + 3) * 1024); \
      ac[t] = mfma(ring[(16 + 24 * (t) + p_) % RD], bq_[p_ & 3], ac[t]); RLOAD(16 + 24 * (t) + p_ + RD); SB(); } } while (0)
#define FINISH(t, pos_) do { ac[t] = mfma32(ab1[t], bop, ac[t]); \
    _Pragma("unroll") for (int e = 0; e < 16; ++e) { const float p_ = vmax(ac[t][e], 0.f); ac[t][e] = (p_ * p_) * r2; } \
    *(LAS v4u*)(h1 + (pos_) * 1024) = pack8(ac[t], 0); *(LAS v4u*)(h1 + ((pos_) + 1) * 1024) = pack8(ac[t], 1); } while (0)
    BREST(0);
    BAR();
    float bop, r2;
    {
      LAS f32x4* sp = (LAS f32x4*)(sm + K2_ST + r * 32);
      const f32x4 st = sp[0] + sp[1];
      const float mean = (st[0] + st[2]) * (1.0f / 512.0f);
      const float var = vmax((st[1] + st[3]) * (1.0f / 512.0f) - mean * mean, 0.f) + 1e-5f;
      const float rstd = __builtin_amdgcn_rsqf(var);
      bop = hh ? var * rstd : -mean;
      r2 = rstd * rstd;
    }
    FINISH(0, 2 * w);
    LGKM0();
    BAR();
    BREST(1);
    FINISH(1, 8 + 2 * w);
    LGKM0();
    BAR();
    if (three) {
      BREST(2);
      FINISH(2, 64 + 2 * (4 * w + j));
    }
    LGKM0();
    BAR();
    BAR();
    BAR();
#undef RLOAD
#undef RLOADS
  }
}
}

extern "C" void kernel_launch(void* const* d_in, const int* in_sizes, int n_in, void* d_out, int out_size, void* d_ws, size_t ws_size,
                              hipStream_t stream) {
  const unsigned short* x = (const unsigned short*)d_in[0];
  const unsigned short* wqkv = (const unsigned short*)d_in[1];
  const unsigned short* wphi = (const unsigned short*)d_in[2];
  const unsigned short* wo = (const unsigned short*)d_in[3];
  const unsigned short* w1 = (const unsigned short*)d_in[5];
  const unsigned short* b1 = (const unsigned short*)d_in[6];
  const unsigned short* w2 = (const unsigned short*)d_in[7];
  const unsigned short* b2 = (const unsigned short*)d_in[8];
  char* ws = (char*)d_ws;
  (void)hipFuncSetAttribute((const void*)k1, hipFuncAttributeMaxDynamicSharedMemorySize, K1_LDS);
  (void)hipFuncSetAttribute((const void*)k2, hipFuncAttributeMaxDynamicSharedMemorySize, K2_LDS);
  k1<<<256, 512, K1_LDS, stream>>>(x, wqkv, wphi, wo, w1, w2, ws);
  k2<<<256, 512, K2_LDS, stream>>>(x, b1, b2, ws, (unsigned short*)d_out);
}
```
